```python
import torch

def _softmax(x, axis=-1):
    return torch.softmax(x, dim=axis)
def _gelu(x):
    return torch.nn.functional.gelu(x, approximate="tanh")
def _silu(x):
    return torch.nn.functional.silu(x)
def _sigmoid(x):
    return torch.sigmoid(x)
def _layernorm(x, axis=-1, eps=1e-5):
    m = x.mean(dim=axis, keepdim=True)
    v = x.var(dim=axis, keepdim=True, unbiased=False)
    return (x - m) / torch.sqrt(v + eps)
def _scatter_add(tgt, idx, src, axis=0):
    return tgt.clone().index_add_(axis, idx, src)
def _segment_sum(src, seg, n):
    out = torch.zeros((n,) + src.shape[1:], dtype=src.dtype)
    return out.index_add_(0, seg, src)
def _pad(x, pw):
    flat = tuple(v for pair in reversed(pw) for v in pair)
    return torch.nn.functional.pad(x, flat)

kernel_name = 'standalone__ssm_conv_s4d__prompt'

def setup_inputs():

    _w = (lambda a: (lambda t: t.to(torch.bfloat16) if t.is_floating_point() else t)(torch.as_tensor(a)))
    Bsz, L, D, N = 1, 2048, 640, 192
    return {
        "u": _w((lambda *s: torch.randn(*s, dtype=torch.bfloat16) * 0.05)(Bsz, L, D)),
        "log_A_real": _w((lambda *s: torch.randn(*s, dtype=torch.bfloat16) * 6.4)(D, N) * 0.5),
        "A_imag": _w((lambda *s: torch.randn(*s, dtype=torch.bfloat16) * 0.05)(D, N) * 3.14),
        "C_real": _w((lambda *s: torch.randn(*s, dtype=torch.bfloat16) * 0.05)(D, N) / N ** 0.5),
        "C_imag": _w((lambda *s: torch.randn(*s, dtype=torch.bfloat16) * 0.05)(D, N) / N ** 0.5),
        "log_dt": _w((lambda *s: torch.randn(*s, dtype=torch.bfloat16) * 0.05)(D) * 0.5 - 4.0),
        "D_skip": _w((lambda *s: torch.randn(*s, dtype=torch.bfloat16) * 0.05)(D)),
    }

def reference(u, log_A_real, A_imag, C_real, C_imag, log_dt, D_skip):
    L = u.shape[1]
    dt = torch.exp(log_dt)
    A = -torch.exp(log_A_real) + 1j * A_imag
    C = (C_real + 1j * C_imag) * dt[:, None]
    pos = (lambda x, dt: x.to(dt))(torch.arange(L), log_dt.dtype)
    P = torch.exp((A * dt[:, None])[:, :, None] * pos[None, None, :])
    K = 2.0 * torch.einsum("dn,dnl->dl", C, P).real
    L2 = 2 * L
    u_t = (lambda x, *d: x.permute(*d))(u, 0, 2, 1)
    Kf = (lambda x, *a, **k: torch.fft.rfft(x.float(), *a, **k))(K, L2)
    Uf = (lambda x, *a, **k: torch.fft.rfft(x.float(), *a, **k))(u_t, L2)
    y = (lambda x, *a, **k: torch.fft.irfft(x, *a, **k).to(torch.bfloat16))(Uf * Kf[None, :, :], L2)[:, :, :L]
    y = (lambda x, *d: x.permute(*d))(y, 0, 2, 1)
    return y + u * D_skip[None, None, :]
```

```cpp
#include <hip/hip_runtime.h>
#include <hip/hip_bf16.h>
#include <stdint.h>

#define L_ 2048
#define D_ 640
#define N_ 192

__device__ __forceinline__ float bf2f(unsigned short h) { return __uint_as_float(((unsigned)h) << 16); }
__device__ __forceinline__ unsigned short f2bf(float f) { __hip_bfloat16 b = __float2bfloat16(f); return *(unsigned short*)&b; }
__device__ __forceinline__ float rbf(float f) { return bf2f(f2bf(f)); }

__global__ void kgen(const unsigned short* lar, const unsigned short* ai, const unsigned short* cr, const unsigned short* ci,
                     const unsigned short* ldt, float* K) {
    int d = blockIdx.x;
    __shared__ float sar[N_], sai[N_], scr[N_], sci[N_];
    float dt = rbf(expf(bf2f(ldt[d])));
    for (int n = threadIdx.x; n < N_; n += blockDim.x) {
        float Ar = -rbf(expf(bf2f(lar[d * N_ + n])));
        float Ai = bf2f(ai[d * N_ + n]);
        sar[n] = Ar * dt; sai[n] = Ai * dt;
        scr[n] = bf2f(cr[d * N_ + n]) * dt; sci[n] = bf2f(ci[d * N_ + n]) * dt;
    }
    __syncthreads();
    for (int l = threadIdx.x; l < L_; l += blockDim.x) {
        float p = rbf((float)l);
        float acc = 0.f;
        for (int n = 0; n < N_; n++) {
            float e = expf(sar[n] * p);
            float s, c; sincosf(sai[n] * p, &s, &c);
            acc += scr[n] * e * c - sci[n] * e * s;
        }
        K[d * L_ + l] = rbf(2.f * acc);
    }
}

__global__ void conv(const unsigned short* u, const float* K, const unsigned short* dsk, unsigned short* out) {
    int d = blockIdx.x;
    __shared__ float su[L_], sk[L_];
    for (int l = threadIdx.x; l < L_; l += blockDim.x) { su[l] = bf2f(u[l * D_ + d]); sk[l] = K[d * L_ + l]; }
    __syncthreads();
    float ds = bf2f(dsk[d]);
    for (int l = threadIdx.x; l < L_; l += blockDim.x) {
        float acc = 0.f;
        for (int j = 0; j <= l; j++) acc += sk[j] * su[l - j];
        out[l * D_ + d] = f2bf(rbf(acc) + rbf(su[l] * ds));
    }
}

extern "C" void kernel_launch(void* const* d_in, const int* in_sizes, int n_in,
                              void* d_out, int out_size, void* d_ws, size_t ws_size,
                              hipStream_t stream) {
    const unsigned short* u = (const unsigned short*)d_in[0];
    float* K = (float*)d_ws;
    kgen<<<D_, 256, 0, stream>>>((const unsigned short*)d_in[1], (const unsigned short*)d_in[2], (const unsigned short*)d_in[3],
                                 (const unsigned short*)d_in[4], (const unsigned short*)d_in[5], K);
    conv<<<D_, 256, 0, stream>>>(u, K, (const unsigned short*)d_in[6], (unsigned short*)d_out);
}
```

```cpp
#include <hip/hip_runtime.h>
#include <stdint.h>

typedef _Float16 h2 __attribute__((ext_vector_type(2)));
typedef _Float16 h8 __attribute__((ext_vector_type(8)));
typedef __bf16 b2 __attribute__((ext_vector_type(2)));
typedef __bf16 b8 __attribute__((ext_vector_type(8)));
typedef float f2 __attribute__((ext_vector_type(2)));
typedef float f4 __attribute__((ext_vector_type(4)));
typedef unsigned u2 __attribute__((ext_vector_type(2)));
typedef unsigned u4 __attribute__((ext_vector_type(4)));
typedef short s4 __attribute__((ext_vector_type(4)));
#define AS3 __attribute__((address_space(3)))
#define DEV __device__ __forceinline__
#define SB() __builtin_amdgcn_sched_barrier(0)

#define NN 192
#define ITERS 3

#define T_STRIDE 4672
#define U_OFF 37376
#define U_STRIDE 4640
#define FG_OFF 74496
#define FG_STRIDE 7696
#define G_REL 2816
#define LDS_BYTES (FG_OFF + 8 * FG_STRIDE)

#define STAMP(i)

template <class T> DEV T ldsr(unsigned a) { return *(const AS3 T*)(uintptr_t)(a); }
template <class T> DEV T ldsrv(unsigned a) { return *(const volatile AS3 T*)(uintptr_t)(a); }
template <class T> DEV void ldsw(unsigned a, T v) { *(AS3 T*)(uintptr_t)(a) = v; }
DEV u2 ldtr(unsigned a) { s4 v = __builtin_amdgcn_ds_read_tr16_b64_v4i16((AS3 s4*)(uintptr_t)(a)); return __builtin_bit_cast(u2, v); }
DEV unsigned opaque(unsigned x) { asm volatile("" : "+v"(x)); return x; }

DEV unsigned pkbf(float a, float b) { f2 v = {a, b}; return __builtin_bit_cast(unsigned, __builtin_convertvector(v, b2)); }
DEV h2 pkh(float a, float b) { f2 v = {a, b}; return __builtin_convertvector(v, h2); }
DEV h2 cmul(h2 h, h2 e) { h2 t = h * (h2){e.x, e.x}; return __builtin_elementwise_fma((h2){-h.y, h.x}, (h2){e.y, e.y}, t); }
DEV h2 cmulc(h2 h, h2 e) { h2 t = h * (h2){e.x, e.x}; return __builtin_elementwise_fma((h2){h.y, -h.x}, (h2){e.y, e.y}, t); }
DEV f4 mfb(u4 a, u4 b, f4 c) { return __builtin_amdgcn_mfma_f32_16x16x32_bf16(__builtin_bit_cast(b8, a), __builtin_bit_cast(b8, b), c, 0, 0, 0); }
DEV f4 mfh(u4 a, u4 b, f4 c) { return __builtin_amdgcn_mfma_f32_16x16x32_f16(__builtin_bit_cast(h8, a), __builtin_bit_cast(h8, b), c, 0, 0, 0); }
DEV u4 cat(u2 a, u2 b) { return (u4){a.x, a.y, b.x, b.y}; }
DEV float fmul(float a, float b) { return a * b; }

#define TW(row, val) asm volatile("ds_write_addtid_b32 %0 offset:%1" :: "v"(val), "i"((row) * 288) : "memory")

DEV void csq(float& re, float& im) { float t = fmul(im, im); float d = re + re; im = fmul(d, im); re = __builtin_fmaf(re, re, -t); }

DEV u4 band(int b, int g8, int lo, int len, bool sel) {
    unsigned by = ((((1u << len) - 1u) << lo) << b >> g8) & 0xffu;
    unsigned e = by | (by << 15);
    u4 r;
#pragma unroll
    for (int m = 0; m < 4; m++) r[m] = opaque(sel ? ((e >> (2 * m)) & 0x10001u) : __umul24(e & (0x10001u << (2 * m)), 0x3F80u >> (2 * m)));
    return r;
}

DEV void put4(unsigned a, unsigned cs, unsigned p01, unsigned p23) {
    unsigned p12 = __builtin_amdgcn_alignbit(p23, p01, 16);
    ldsw<u2>(a, (u2){p01, p23});
    ldsw<unsigned short>(a + cs + 2, (unsigned short)p01);
    ldsw<unsigned>(a + cs + 4, p12);
    ldsw<unsigned short>(a + cs + 8, (unsigned short)(p23 >> 16));
    ldsw<unsigned>(a + 2 * cs + 4, p01);
    ldsw<unsigned>(a + 2 * cs + 8, p23);
    ldsw<unsigned short>(a + 3 * cs + 6, (unsigned short)p01);
    ldsw<unsigned>(a + 3 * cs + 8, p12);
    ldsw<unsigned short>(a + 3 * cs + 12, (unsigned short)(p23 >> 16));
}

struct Ctx {
    unsigned uf;
    unsigned ut;
    unsigned wt;
    unsigned ww;
    unsigned st;
    u4 B1, B2a, B2b, B3, B4, BF;
    u4 F[9];
    u4 L1[2][4];
    u4 L2[2][2];
    u4 L3[4];
};

DEV u4 ufrag(const Ctx& c, int S, int s0) { return ldsr<u4>(c.uf + 512 * S + 2 * (266 - s0)); }
DEV u2 gq(unsigned base, int ytop, int dl) { return ldsrv<u2>(base + 2 * (ytop - 3 - 16 * dl)); }
#define PP(ph) ((ph) == 18 ? 0 : (ph) / 2 - 1)
#define YT(ph) (112 * (PP(ph) >> 1) + ((PP(ph) & 1) ? 111 : 47))
#define YH8 (448 + 127)
#define YH16 (528 + 127)
DEV u4 gfrag(unsigned ga, int y1, int y2, int dl) { return cat(gq(ga, y1, dl), gq(ga, y2, dl)); }
DEV u2 cv4(f4 d) { return (u2){pkbf(d[0], d[1]), pkbf(d[2], d[3])}; }
DEV u4 utr(const Ctx& c, int S, int p1, int p2) { return cat(ldtr(c.ut + 512 * S + 2 * (266 - p1)), ldtr(c.ut + 512 * S + 2 * (266 - p2))); }
DEV u4 wtr(const Ctx& c, int Sr, int p1, int p2) { return cat(ldtr(c.wt + 512 * Sr + 2 * (16 - p1)), ldtr(c.wt + 512 * Sr + 2 * (16 - p2))); }

template <int R> DEV void conv_far(const Ctx& c) {
    const f4 z = {0.f, 0.f, 0.f, 0.f};
    constexpr int SM = R - 4;
    u4 u18[8], u26[4], u10[2], fu[17];
#pragma unroll
    for (int S = SM; S <= R + 1; S++) u18[S] = ufrag(c, S, 18);
#pragma unroll
    for (int i = 0; i < 9; i++) fu[i] = ufrag(c, R, 266 - 32 * i);
    SB();
    {
        f4 d[6];
#pragma unroll
        for (int S = 0; S < 6; S++) d[S] = mfb(c.B1, u18[SM + S], z);
#pragma unroll
        for (int S = 0; S < 6; S++) ldsw<u2>(c.ww + 512 * S, cv4(d[S]));
    }
#pragma unroll
    for (int i = 9; i < 17; i++) fu[i] = ufrag(c, R, 266 - 32 * i);
    SB();
    f4 a0 = z, a1 = z;
#pragma unroll
    for (int i = 0; i < 9; i++) a0 = mfb(c.F[8 - i], fu[i], a0);
#pragma unroll
    for (int S = 0; S < SM; S++) u18[S] = ufrag(c, S, 18);
#pragma unroll
    for (int S = 0; S <= R - 3; S++) u26[S] = ufrag(c, S, 26);
    u10[0] = ufrag(c, R - 4, 10); u10[1] = ufrag(c, R - 3, 10);
    SB();
#pragma unroll
    for (int i = 0; i < 9; i++) a1 = mfb(c.F[8 - i], fu[8 + i], a1);
    u4 va[3], vb[3], vc[5], vd[3];
#pragma unroll
    for (int s = 0; s < 3; s++) { va[s] = utr(c, R - 2 + s, 18, 6); vb[s] = utr(c, R - 2 + s, 10, 14); }
    SB();
    u4 v2[3], v3[4];
#pragma unroll
    for (int s = 0; s < 3; s++) v2[s] = cat(cv4(mfb(u18[SM + s], c.B2b, z)), cv4(mfb(u18[SM + s], c.B2a, z)));
#pragma unroll
    for (int s = 0; s < 5; s++) vc[s] = wtr(c, s, 4, 12);
#pragma unroll
    for (int s = 0; s < 3; s++) vd[s] = wtr(c, 2 + s, 8, 16);
    SB();
#pragma unroll
    for (int S = 0; S <= R - 3; S++) v3[S] = cat(cv4(mfb(u18[S], c.B3, z)), cv4(mfb(u26[S], c.B4, z)));
    a0 = mfb(c.BF, u10[0], a0);
    a1 = mfb(c.BF, u10[1], a1);
    a0 = mfb(va[0], c.L1[1][0], a0); a1 = mfb(va[1], c.L1[1][0], a1);
    a0 = mfb(vb[0], c.L1[1][1], a0); a1 = mfb(vb[1], c.L1[1][1], a1);
    a0 = mfb(va[1], c.L1[0][0], a0); a1 = mfb(va[2], c.L1[0][0], a1);
    a0 = mfb(vb[1], c.L1[0][1], a0); a1 = mfb(vb[2], c.L1[0][1], a1);
    SB();
    a0 = mfb(v2[0], c.L2[1][1], a0); a1 = mfb(v2[1], c.L2[1][1], a1);
    a0 = mfb(v2[1], c.L2[0][1], a0); a1 = mfb(v2[2], c.L2[0][1], a1);
    a0 = mfb(vc[0], c.L2[1][0], a0); a1 = mfb(vc[1], c.L2[1][0], a1);
    a0 = mfb(vc[1], c.L2[0][0], a0); a1 = mfb(vc[2], c.L2[0][0], a1);
    a0 = mfb(vc[2], c.L1[1][2], a0); a1 = mfb(vc[3], c.L1[1][2], a1);
    a0 = mfb(vc[3], c.L1[0][2], a0); a1 = mfb(vc[4], c.L1[0][2], a1);
    a0 = mfb(vd[0], c.L1[1][3], a0); a1 = mfb(vd[1], c.L1[1][3], a1);
    a0 = mfb(vd[1], c.L1[0][3], a0); a1 = mfb(vd[2], c.L1[0][3], a1);
#pragma unroll
    for (int S = 0; S <= R - 3; S++) {
        if (R - S >= 4) a0 = mfb(v3[S], c.L3[R - S - 4], a0);
        a1 = mfb(v3[S], c.L3[R - S - 3], a1);
    }
    ldsw<u2>(c.st, cv4(a0));
    ldsw<u2>(c.st + 512, cv4(a1));
}

DEV void conv_near(const Ctx& c) {
    const f4 z = {0.f, 0.f, 0.f, 0.f};
    u4 u18[4], fu[33];
#pragma unroll
    for (int S = 0; S < 4; S++) u18[S] = ufrag(c, S, 18);
#pragma unroll
    for (int i = 0; i < 9; i++) fu[i] = ufrag(c, 0, 266 - 32 * i);
    SB();
    u4 v2;
    {
        f4 d[4];
#pragma unroll
        for (int S = 0; S < 4; S++) d[S] = mfb(c.B1, u18[S], z);
        v2 = cat(cv4(mfb(u18[0], c.B2b, z)), cv4(mfb(u18[0], c.B2a, z)));
#pragma unroll
        for (int S = 0; S < 4; S++) ldsw<u2>(c.ww + 512 * S, cv4(d[S]));
    }
#pragma unroll
    for (int i = 9; i < 17; i++) fu[i] = ufrag(c, 0, 266 - 32 * i);
    SB();
    f4 a0 = z, a1 = z, a2 = z, a3 = z;
#pragma unroll
    for (int i = 0; i < 9; i++) a0 = mfb(c.F[8 - i], fu[i], a0);
#pragma unroll
    for (int i = 17; i < 25; i++) fu[i] = ufrag(c, 0, 266 - 32 * i);
    SB();
#pragma unroll
    for (int i = 0; i < 9; i++) a1 = mfb(c.F[8 - i], fu[8 + i], a1);
    ldsw<u2>(c.st, cv4(a0));
#pragma unroll
    for (int i = 25; i < 33; i++) fu[i] = ufrag(c, 0, 266 - 32 * i);
    SB();
#pragma unroll
    for (int i = 0; i < 9; i++) a2 = mfb(c.F[8 - i], fu[16 + i], a2);
    u4 va[3], vb[3], vc[3], vd[3];
#pragma unroll
    for (int s = 0; s < 3; s++) { va[s] = utr(c, s, 18, 6); vb[s] = utr(c, s, 10, 14); }
    SB();
#pragma unroll
    for (int i = 0; i < 9; i++) a3 = mfb(c.F[8 - i], fu[24 + i], a3);
#pragma unroll
    for (int s = 0; s < 3; s++) { vc[s] = wtr(c, s, 4, 12); vd[s] = wtr(c, s, 8, 16); }
    SB();
    a1 = mfb(va[0], c.L1[0][0], a1); a2 = mfb(va[1], c.L1[0][0], a2); a3 = mfb(va[2], c.L1[0][0], a3);
    a1 = mfb(vb[0], c.L1[0][1], a1); a2 = mfb(vb[1], c.L1[0][1], a2); a3 = mfb(vb[2], c.L1[0][1], a3);
    a2 = mfb(va[0], c.L1[1][0], a2); a3 = mfb(va[1], c.L1[1][0], a3);
    a2 = mfb(vb[0], c.L1[1][1], a2); a3 = mfb(vb[1], c.L1[1][1], a3);
    a3 = mfb(v2, c.L2[0][1], a3);
    a1 = mfb(vc[0], c.L1[0][2], a1); a2 = mfb(vc[1], c.L1[0][2], a2); a3 = mfb(vc[2], c.L1[0][2], a3);
    a1 = mfb(vd[0], c.L1[0][3], a1); a2 = mfb(vd[1], c.L1[0][3], a2); a3 = mfb(vd[2], c.L1[0][3], a3);
    a2 = mfb(vc[0], c.L1[1][2], a2); a3 = mfb(vc[1], c.L1[1][2], a3);
    a2 = mfb(vd[0], c.L1[1][3], a2); a3 = mfb(vd[1], c.L1[1][3], a3);
    a3 = mfb(vc[0], c.L2[0][0], a3);
    ldsw<u2>(c.st + 512, cv4(a1));
    ldsw<u2>(c.st + 1024, cv4(a2));
    ldsw<u2>(c.st + 1536, cv4(a3));
}

__global__ __launch_bounds__(512, 2) void s4d_kernel(const unsigned short* __restrict__ u, const unsigned short* __restrict__ lar,
                                                     const unsigned short* __restrict__ aim, const unsigned short* __restrict__ cre,
                                                     const unsigned short* __restrict__ cim, const unsigned short* __restrict__ ldt,
                                                     const unsigned short* __restrict__ dsk, unsigned short* __restrict__ out
) {
    __shared__ __attribute__((aligned(16))) char smem[LDS_BYTES];
    const unsigned sb = (unsigned)(uintptr_t)(AS3 char*)smem;
    STAMP(0);
    const int tid = threadIdx.x;
    const int lane = tid & 63;
    const int w = __builtin_amdgcn_readfirstlane(tid >> 6);
    const int bx = blockIdx.x;
    const int xi = bx >> 3;
    const int part = xi / 10;
    const int grp = (bx & 7) * 10 + (xi - part * 10);
    const int ch = grp * 8 + w;
    const int nrows = 2048 - part * 512;

    const unsigned po = ch * NN;
    unsigned ldw = ((const unsigned*)ldt)[ch >> 1];
    unsigned dkw = ((const unsigned*)dsk)[ch >> 1];
    unsigned pl01 = *(const unsigned*)(lar + po + 2 * lane);
    unsigned pa01 = *(const unsigned*)(aim + po + 2 * lane);
    unsigned pr01 = *(const unsigned*)(cre + po + 2 * lane);
    unsigned pi01 = *(const unsigned*)(cim + po + 2 * lane);
    unsigned pr2 = (cre + po + 128)[lane];
    unsigned pi2 = (cim + po + 128)[lane];
    unsigned pl2 = (lar + po + 128)[lane];
    unsigned pa2 = (aim + po + 128)[lane];
    SB();
    unsigned pf;
    {
        auto rs = __builtin_amdgcn_make_buffer_rsrc((void*)u, (short)0, 2048 * 1280, 0x00020000);
        unsigned idx = xi * 137 + tid;
        unsigned off = (idx >> 1) * 1280 + (bx & 7) * 160 + (idx & 1) * 156;
        pf = __builtin_amdgcn_raw_buffer_load_b32(rs, tid < 137 ? off : 0x7fffff00u, 0, 0);
    }
    u4 ur[4];
    {
        auto rs = __builtin_amdgcn_make_buffer_rsrc((void*)u, (short)0, nrows * 1280, 0x00020000);
#pragma unroll
        for (int k = 0; k < 4; k++) ur[k] = __builtin_amdgcn_raw_buffer_load_b128(rs, tid * 5120 + k * 1280, grp * 16, 0);
    }
    SB();

    const unsigned tb = sb + w * T_STRIDE;
    const unsigned ub = sb + U_OFF + w * U_STRIDE;
    const unsigned fgb = sb + FG_OFF + w * FG_STRIDE;
    {
        const u4 z4 = {0u, 0u, 0u, 0u};
#pragma unroll
        for (int k = 0; k < 7; k++) ldsw<u4>(fgb + k * 1024 + lane * 16, z4);
        if (lane < 33) { ldsw<u4>(fgb + 7168 + lane * 16, z4); ldsw<u4>(ub + lane * 16, z4); }
        if (lane < 4) ldsw<unsigned>(lane == 3 ? ub + 528 : ub + 532 + 4096 + lane * 4, 0u);
    }
    const int la = lane & 15, lg = lane >> 4;
    Ctx c;
    c.B1 = band(la, 8 * lg, 1, 3, false);
    c.B2a = band(la, 8 * lg, 0, 5, false);
    c.B2b = band(la, 8 * lg, 8, 5, false);
    c.B3 = band(la, 8 * lg, 7, 7, false);
    c.B4 = band(la, 8 * lg, 6, 9, false);
    c.BF = band(la, 8 * lg, 6, 2, true);
    c.uf = opaque(ub + 32 * la + 16 * lg);
    c.ut = opaque(ub + 8 * lane);
    c.wt = opaque(tb + 8 * lane);
    c.ww = opaque(tb + 32 * la + 8 * lg);
    c.st = opaque(c.ww + (part == 2 ? 2048 : 3072));
    const unsigned fa = opaque(fgb + 706 * ((la + 3) & 3) - 2 * la + 16 * lg + 2);
    const unsigned ga = opaque(fgb + G_REL + 1218 * ((la + 1) & 3) - 2 * la + 8 * lg + 6);
    const unsigned tr = opaque(tb + 288 * la + 16 * lg);
    const unsigned wf = opaque(fgb + 2 * (28 + 16 * la + 4 * lg));
    const unsigned wg1 = opaque(fgb + G_REL + 2 * (112 * ((la & 7) >> 1) + 16 + 64 * (la & 1) - 16 * (la >> 3) + 4 * lg));
    const unsigned wg2 = opaque(fgb + G_REL + 2 * ((la < 4 ? 112 * la + 48 : 416 + 80 * (la >> 2) - 16 * (la & 3)) + 4 * lg));
    const unsigned wpt = opaque(fgb + (la < 12 ? 706 * (lane & 3) + 54 - 2 * (la >> 2) : G_REL + 1218 * (lane & 3) + 32));
    const float m63 = __uint_as_float(opaque(lane == 63 ? 0x3f800000u : 0u));
    const unsigned twa = opaque(tb + 4 * lane);
    const unsigned uw = opaque(sb + U_OFF + 532 + 8 * tid);
    asm volatile("s_mov_b32 m0, %0\n s_nop 0" :: "s"(tb) : "memory");
    STAMP(1);

    const float ldtf = __uint_as_float((ch & 1) ? (ldw & 0xffff0000u) : (ldw << 16));
    const float dskf = __uint_as_float((ch & 1) ? (dkw & 0xffff0000u) : (dkw << 16));
    const float LOG2E = 1.4426950408889634f;
    const float dt = __uint_as_float(pkbf(0.f, __builtin_amdgcn_exp2f(ldtf * LOG2E)) & 0xffff0000u);
    const float cm = -dt * LOG2E, cth = dt * 0.15915494309189535f;
    unsigned mx;
    {
        unsigned m0_ = max(pr01 & 0x7fff0000u, pi01 & 0x7fff0000u);
        unsigned m1_ = max(pr01 & 0x7fffu, pi01 & 0x7fffu);
        unsigned m2_ = max(pr2 & 0x7fffu, pi2 & 0x7fffu);
        mx = max(m0_, max(m1_, m2_) << 16);
        mx = max(mx, (unsigned)__builtin_amdgcn_update_dpp(0, (int)mx, 0x111, 0xf, 0xf, true));
        mx = max(mx, (unsigned)__builtin_amdgcn_update_dpp(0, (int)mx, 0x112, 0xf, 0xf, true));
        mx = max(mx, (unsigned)__builtin_amdgcn_update_dpp(0, (int)mx, 0x114, 0xf, 0xf, true));
        mx = max(mx, (unsigned)__builtin_amdgcn_update_dpp(0, (int)mx, 0x118, 0xf, 0xf, true));
        unsigned q0 = __builtin_amdgcn_readlane(mx, 15), q1 = __builtin_amdgcn_readlane(mx, 31);
        unsigned q2 = __builtin_amdgcn_readlane(mx, 47), q3 = __builtin_amdgcn_readlane(mx, 63);
        mx = max(max(q0, q1), max(q2, q3));
    }
    const unsigned ex = max(mx >> 23, 16u);
    const float sc = __uint_as_float((268u - ex) << 23);
    const float ks = dt * __uint_as_float((ex - 13u) << 23);
    STAMP(2);

    f4 acc0 = {0.f, 0.f, 0.f, 0.f}, acc1 = acc0, acc2 = acc0;
    unsigned cl = pl01 << 16, ca = pa01 << 16, cr = pr01 << 16, ci = pi01 << 16;
    unsigned nl = pl01 & 0xffff0000u, na = pa01 & 0xffff0000u, nr = pr01 & 0xffff0000u, ni = pi01 & 0xffff0000u;
#pragma unroll 1
    for (int gi = 0; gi < ITERS; gi++) {
        const float arp = __uint_as_float(pkbf(0.f, __builtin_amdgcn_exp2f(__uint_as_float(cl) * LOG2E)) & 0xffff0000u);
        const float m1 = arp * cm;
        const float tt = __uint_as_float(ca) * cth;
        const float th = __builtin_amdgcn_fractf(tt);
        const float th2 = __builtin_amdgcn_fractf(tt * 16.f);
        float mg2 = __builtin_amdgcn_exp2f(m1 * 16.f);
        float re2 = fmul(mg2, __builtin_amdgcn_cosf(th2)), im2 = fmul(mg2, __builtin_amdgcn_sinf(th2));
        const h2 x0 = pkh(fmul(__uint_as_float(cr), sc), fmul(__uint_as_float(ci), -sc));
        const h2 e16 = pkh(re2, im2); csq(re2, im2);
        const h2 e32 = pkh(re2, im2); csq(re2, im2);
        const h2 e64 = pkh(re2, im2); csq(re2, im2);
        const h2 e128 = pkh(re2, im2);
        SB();
        const h2 x1 = cmulc(x0, e16), x2 = cmulc(x0, e32); TW(15, x0); SB();
        const h2 x3 = cmulc(x1, e32), x4 = cmulc(x0, e64); TW(14, x1); TW(13, x2); SB();
        const h2 x5 = cmulc(x1, e64), x6 = cmulc(x2, e64); TW(12, x3); TW(11, x4); SB();
        const h2 x7 = cmulc(x3, e64); h2 t0 = cmulc(x0, e128); TW(10, x5); TW(9, x6); SB();
        h2 t1 = cmulc(x1, e128), t2 = cmulc(x2, e128); TW(8, x7); TW(7, t0); SB();
        h2 t3 = cmulc(x3, e128); t0 = cmulc(x4, e128); TW(6, t1); TW(5, t2); SB();
        t1 = cmulc(x5, e128); t2 = cmulc(x6, e128); TW(4, t3); TW(3, t0); SB();
        t3 = cmulc(x7, e128);
        float mg = __builtin_amdgcn_exp2f(m1);
        float re = fmul(mg, __builtin_amdgcn_cosf(th)), im = fmul(mg, __builtin_amdgcn_sinf(th));
        TW(2, t1); TW(1, t2); SB();
        TW(0, t3);
        const h2 e1 = pkh(re, im); csq(re, im);
        u4 xf[4];
#pragma unroll
        for (int s = 0; s < 4; s++) xf[s] = ldsr<u4>(tr + 64 * s);
        const h2 e2 = pkh(re, im); csq(re, im);
        SB();
        const h2 one = {(_Float16)1.f, (_Float16)0.f};
        const h2 e4 = pkh(re, im); csq(re, im);
        TW(15, one); TW(14, e1); SB();
        const h2 e8 = pkh(re, im);
        t0 = cmul(e2, e1); t1 = cmul(e4, e1); TW(13, e2); TW(11, e4); SB();
        const h2 y6 = cmul(e4, e2); t2 = cmul(e8, e1); TW(12, t0); TW(10, t1); TW(7, e8); SB();
        const h2 y10 = cmul(e8, e2), y12 = cmul(e8, e4); TW(9, y6); TW(6, t2); SB();
        t0 = cmul(y6, e1); const h2 y14 = cmul(e8, y6); TW(5, y10); TW(3, y12); SB();
        t1 = cmul(y10, e1); t2 = cmul(y12, e1); TW(8, t0); TW(1, y14); SB();
        t3 = cmul(y14, e1); csq(re2, im2); TW(4, t1); TW(2, t2); SB();
        TW(0, t3);
        const h2 e256 = pkh(re2, im2); csq(re2, im2);
        u4 yf[4];
#pragma unroll
        for (int s = 0; s < 4; s++) yf[s] = ldsr<u4>(tr + 64 * s);
        const h2 e512 = pkh(re2, im2); csq(re2, im2);
        const h2 p272 = cmul(e256, e16); t0 = cmul(e256, e4);
        SB();
        t1 = cmul(e256, y6); const h2 p264 = cmul(e256, e8); TW(7, p272); TW(1, t0); SB();
        t2 = cmul(e256, y10); t3 = cmul(e256, y12); TW(2, t1); TW(3, p264); SB();
        acc0 = mfh(yf[0], xf[0], acc0);
        t0 = cmul(e256, y14); t1 = cmul(p272, e2); TW(4, t2); TW(5, t3); SB();
        acc0 = mfh(yf[1], xf[1], acc0);
        const h2 p520 = cmul(e512, e8), p528 = cmul(e512, e16); TW(6, t0); TW(0, t1); SB();
        acc0 = mfh(yf[2], xf[2], acc0);
        const h2 p516 = cmul(e512, e4), p524 = cmul(e512, y12); TW(10, p520); TW(11, p520); TW(12, p520); SB();
        acc0 = mfh(yf[3], xf[3], acc0);
        const h2 e1024 = pkh(re2, im2);
        const h2 p776 = cmul(e256, p520); TW(8, p528); TW(14, p528); TW(15, p528); SB();
        const h2 p784 = cmul(e256, p528); t0 = cmul(e256, p516); TW(9, p516); TW(13, p524);
#pragma unroll
        for (int s = 0; s < 4; s++) yf[s] = ldsr<u4>(tr + 64 * s);
        SB();
        t1 = cmul(e256, p524); t2 = cmul(e1024, e8); TW(1, p776); TW(3, p784); TW(0, t0); SB();
        t3 = cmul(e1024, p264); t0 = cmul(e1024, p520); TW(2, t1); TW(4, t2); SB();
        acc1 = mfh(xf[0], yf[0], acc1);
        t1 = cmul(e1024, p776); t2 = cmul(e1024, e16); TW(5, t3); TW(6, t0); SB();
        acc1 = mfh(xf[1], yf[1], acc1);
        t3 = cmul(e1024, p272); t0 = cmul(e1024, p528); TW(7, t1); TW(8, t2); SB();
        acc1 = mfh(xf[2], yf[2], acc1);
        t1 = cmul(e1024, p784); t2 = cmul(e256, e2); TW(9, t3); TW(10, t0); TW(12, e256); SB();
        acc1 = mfh(xf[3], yf[3], acc1);
        TW(11, t1); TW(13, t2);
#pragma unroll
        for (int s = 0; s < 4; s++) yf[s] = ldsr<u4>(tr + 64 * s);
        cl = nl; ca = na; cr = nr; ci = ni;
        nl = pl2 << 16; na = pa2 << 16; nr = pr2 << 16; ni = pi2 << 16;
#pragma unroll
        for (int s = 0; s < 4; s++) acc2 = mfh(xf[s], yf[s], acc2);
    }
    STAMP(3);
    {
        float k3 = __builtin_fmaf(m63, dskf, fmul(acc0[3], ks));
        put4(wf, 704, pkbf(fmul(acc0[0], ks), fmul(acc0[1], ks)), pkbf(fmul(acc0[2], ks), k3));
        unsigned r01 = pkbf(fmul(acc1[0], ks), fmul(acc1[1], ks));
        put4(wg1, 1216, r01, pkbf(fmul(acc1[2], ks), fmul(acc1[3], ks)));
        unsigned k512 = __builtin_amdgcn_readlane(r01, 7);
        unsigned q01 = pkbf(fmul(acc2[0], ks), fmul(acc2[1], ks)), q23 = pkbf(fmul(acc2[2], ks), fmul(acc2[3], ks));
        if (la < 12) put4(wg2, 1216, q01, q23);
        unsigned k256 = __builtin_amdgcn_readlane(q23, 60) >> 16;
        unsigned k258 = __builtin_amdgcn_readlane(q23, 61) >> 16;
        unsigned k1024 = __builtin_amdgcn_readlane(q01, 3) & 0xffffu;
        asm volatile("" ::: "memory");
        if (lane < 16) ldsw<unsigned short>(wpt, (unsigned short)(lane < 8 ? k256 : (lane < 12 ? k258 : k512)));
#pragma unroll
        for (int m = 0; m < 4; m++) c.BF[m] = __umul24(c.BF[m], k1024);
    }
    asm volatile("" ::: "memory");
#pragma unroll
    for (int i = 0; i < 9; i++) c.F[i] = cat(ldsrv<u2>(fa + 544 - 64 * i), ldsrv<u2>(fa + 552 - 64 * i));
#pragma unroll
    for (int d = 0; d < 2; d++) {
        c.L1[d][0] = gfrag(ga, YT(18), YT(6), d + 1);
        c.L1[d][1] = gfrag(ga, YT(10), YT(14), d + 1);
        c.L1[d][2] = gfrag(ga, YT(4), YT(12), d + 1);
        c.L1[d][3] = gfrag(ga, YT(8), YT(16), d + 1);
        c.L2[d][0] = gfrag(ga, YT(4), YT(12), d + 3);
        c.L2[d][1] = gfrag(ga, YT(8), YT(16), d + 3);
    }
#pragma unroll
    for (int d = 0; d < 4; d++) c.L3[d] = gfrag(ga, YH8, YH16, d + 4);
    STAMP(4);
    asm volatile("" :: "v"(pf));
#pragma unroll
    for (int d = 0; d < 4; d++) {
        unsigned lo01 = __builtin_amdgcn_perm(ur[1][d], ur[0][d], 0x05040100u);
        unsigned lo23 = __builtin_amdgcn_perm(ur[3][d], ur[2][d], 0x05040100u);
        unsigned hi01 = __builtin_amdgcn_perm(ur[1][d], ur[0][d], 0x07060302u);
        unsigned hi23 = __builtin_amdgcn_perm(ur[3][d], ur[2][d], 0x07060302u);
        ldsw<unsigned>(uw + (2 * d) * U_STRIDE, lo01);
        ldsw<unsigned>(uw + (2 * d) * U_STRIDE + 4, lo23);
        ldsw<unsigned>(uw + (2 * d + 1) * U_STRIDE, hi01);
        ldsw<unsigned>(uw + (2 * d + 1) * U_STRIDE + 4, hi23);
    }
    STAMP(5);
    __syncthreads();
    STAMP(6);
    if (part == 0) conv_far<6>(c);
    else if (part == 1) conv_far<4>(c);
    else conv_near(c);
    STAMP(7);
    __syncthreads();
    STAMP(8);
    {
        const unsigned ea = sb + ((lane >> 2) & 3) * T_STRIDE + (part == 2 ? 2048 : 3072) + 2 * (64 * w + 16 * lg + 4 * (lane & 3));
        const int row0 = part == 2 ? 0 : nrows - 512;
        char* op = (char*)out + (size_t)(row0 + tid) * 1280 + grp * 16;
        u4 v = cat(ldtr(ea), ldtr(ea + 4 * T_STRIDE));
        *(u4*)op = v;
        if (part == 2) {
            u4 v2 = cat(ldtr(ea + 1024), ldtr(ea + 1024 + 4 * T_STRIDE));
            *(u4*)(op + 512 * 1280) = v2;
        }
    }
}

extern "C" void kernel_launch(void* const* d_in, const int* in_sizes, int n_in, void* d_out, int out_size, void* d_ws, size_t ws_size,
                              hipStream_t stream) {
    s4d_kernel<<<240, 512, 0, stream>>>((const unsigned short*)d_in[0], (const unsigned short*)d_in[1], (const unsigned short*)d_in[2],
                                        (const unsigned short*)d_in[3], (const unsigned short*)d_in[4], (const unsigned short*)d_in[5],
                                        (const unsigned short*)d_in[6], (unsigned short*)d_out
    );
}
```

```cpp
#define TSPIN 0
#include <hip/hip_runtime.h>
#include <stdint.h>

typedef _Float16 h2 __attribute__((ext_vector_type(2)));
typedef _Float16 h8 __attribute__((ext_vector_type(8)));
typedef __bf16 b2 __attribute__((ext_vector_type(2)));
typedef __bf16 b8 __attribute__((ext_vector_type(8)));
typedef float f2 __attribute__((ext_vector_type(2)));
typedef float f4 __attribute__((ext_vector_type(4)));
typedef unsigned u2 __attribute__((ext_vector_type(2)));
typedef unsigned u4 __attribute__((ext_vector_type(4)));
typedef short s4 __attribute__((ext_vector_type(4)));
#define AS3 __attribute__((address_space(3)))
#define DEV __device__ __forceinline__
#define SB() __builtin_amdgcn_sched_barrier(0)

#define NN 192
#define ITERS 3

#define T_STRIDE 4672
#define U_OFF 37376
#define U_STRIDE 4640
#define FG_OFF 74496
#define FG_STRIDE 7696
#define G_REL 2816
#define LDS_BYTES (FG_OFF + 8 * FG_STRIDE)

#define STAMP(i) do { SB(); ts[i] = __builtin_amdgcn_s_memrealtime(); SB(); } while (0)

template <class T> DEV T ldsr(unsigned a) { return *(const AS3 T*)(uintptr_t)(a); }
template <class T> DEV T ldsrv(unsigned a) { return *(const volatile AS3 T*)(uintptr_t)(a); }
template <class T> DEV void ldsw(unsigned a, T v) { *(AS3 T*)(uintptr_t)(a) = v; }
DEV u2 ldtr(unsigned a) { s4 v = __builtin_amdgcn_ds_read_tr16_b64_v4i16((AS3 s4*)(uintptr_t)(a)); return __builtin_bit_cast(u2, v); }
DEV unsigned opaque(unsigned x) { asm volatile("" : "+v"(x)); return x; }

DEV unsigned pkbf(float a, float b) { f2 v = {a, b}; return __builtin_bit_cast(unsigned, __builtin_convertvector(v, b2)); }
DEV h2 pkh(float a, float b) { f2 v = {a, b}; return __builtin_convertvector(v, h2); }
DEV h2 cmul(h2 h, h2 e) { h2 t = h * (h2){e.x, e.x}; return __builtin_elementwise_fma((h2){-h.y, h.x}, (h2){e.y, e.y}, t); }
DEV h2 cmulc(h2 h, h2 e) { h2 t = h * (h2){e.x, e.x}; return __builtin_elementwise_fma((h2){h.y, -h.x}, (h2){e.y, e.y}, t); }
DEV f4 mfb(u4 a, u4 b, f4 c) { return __builtin_amdgcn_mfma_f32_16x16x32_bf16(__builtin_bit_cast(b8, a), __builtin_bit_cast(b8, b), c, 0, 0, 0); }
DEV f4 mfh(u4 a, u4 b, f4 c) { return __builtin_amdgcn_mfma_f32_16x16x32_f16(__builtin_bit_cast(h8, a), __builtin_bit_cast(h8, b), c, 0, 0, 0); }
DEV u4 cat(u2 a, u2 b) { return (u4){a.x, a.y, b.x, b.y}; }
DEV float fmul(float a, float b) { return a * b; }

#define TW(row, val) asm volatile("ds_write_addtid_b32 %0 offset:%1" :: "v"(val), "i"((row) * 288) : "memory")

DEV void csq(float& re, float& im) { float t = fmul(im, im); float d = re + re; im = fmul(d, im); re = __builtin_fmaf(re, re, -t); }

DEV u4 band(int b, int g8, int lo, int len, bool sel) {
    unsigned by = ((((1u << len) - 1u) << lo) << b >> g8) & 0xffu;
    unsigned e = by | (by << 15);
    u4 r;
#pragma unroll
    for (int m = 0; m < 4; m++) r[m] = opaque(sel ? ((e >> (2 * m)) & 0x10001u) : __umul24(e & (0x10001u << (2 * m)), 0x3F80u >> (2 * m)));
    return r;
}

DEV void put4(unsigned a, unsigned cs, unsigned p01, unsigned p23) {
    unsigned p12 = __builtin_amdgcn_alignbit(p23, p01, 16);
    ldsw<u2>(a, (u2){p01, p23});
    ldsw<unsigned short>(a + cs + 2, (unsigned short)p01);
    ldsw<unsigned>(a + cs + 4, p12);
    ldsw<unsigned short>(a + cs + 8, (unsigned short)(p23 >> 16));
    ldsw<unsigned>(a + 2 * cs + 4, p01);
    ldsw<unsigned>(a + 2 * cs + 8, p23);
    ldsw<unsigned short>(a + 3 * cs + 6, (unsigned short)p01);
    ldsw<unsigned>(a + 3 * cs + 8, p12);
    ldsw<unsigned short>(a + 3 * cs + 12, (unsigned short)(p23 >> 16));
}

struct Ctx {
    unsigned uf;
    unsigned ut;
    unsigned wt;
    unsigned ww;
    unsigned st;
    u4 B1, B2a, B2b, B3, B4, BF;
    u4 F[9];
    u4 L1[2][4];
    u4 L2[2][2];
    u4 L3[4];
};

DEV u4 ufrag(const Ctx& c, int S, int s0) { return ldsr<u4>(c.uf + 512 * S + 2 * (266 - s0)); }
DEV u2 gq(unsigned base, int ytop, int dl) { return ldsrv<u2>(base + 2 * (ytop - 3 - 16 * dl)); }
#define PP(ph) ((ph) == 18 ? 0 : (ph) / 2 - 1)
#define YT(ph) (112 * (PP(ph) >> 1) + ((PP(ph) & 1) ? 111 : 47))
#define YH8 (448 + 127)
#define YH16 (528 + 127)
DEV u4 gfrag(unsigned ga, int y1, int y2, int dl) { return cat(gq(ga, y1, dl), gq(ga, y2, dl)); }
DEV u2 cv4(f4 d) { return (u2){pkbf(d[0], d[1]), pkbf(d[2], d[3])}; }
DEV u4 utr(const Ctx& c, int S, int p1, int p2) { return cat(ldtr(c.ut + 512 * S + 2 * (266 - p1)), ldtr(c.ut + 512 * S + 2 * (266 - p2))); }
DEV u4 wtr(const Ctx& c, int Sr, int p1, int p2) { return cat(ldtr(c.wt + 512 * Sr + 2 * (16 - p1)), ldtr(c.wt + 512 * Sr + 2 * (16 - p2))); }

template <int R> DEV void conv_far(const Ctx& c) {
    const f4 z = {0.f, 0.f, 0.f, 0.f};
    constexpr int SM = R - 4;
    u4 u18[8], u26[4], u10[2], fu[17];
#pragma unroll
    for (int S = SM; S <= R + 1; S++) u18[S] = ufrag(c, S, 18);
#pragma unroll
    for (int i = 0; i < 9; i++) fu[i] = ufrag(c, R, 266 - 32 * i);
    SB();
    {
        f4 d[6];
#pragma unroll
        for (int S = 0; S < 6; S++) d[S] = mfb(c.B1, u18[SM + S], z);
#pragma unroll
        for (int S = 0; S < 6; S++) ldsw<u2>(c.ww + 512 * S, cv4(d[S]));
    }
#pragma unroll
    for (int i = 9; i < 17; i++) fu[i] = ufrag(c, R, 266 - 32 * i);
    SB();
    f4 a0 = z, a1 = z;
#pragma unroll
    for (int i = 0; i < 9; i++) a0 = mfb(c.F[8 - i], fu[i], a0);
#pragma unroll
    for (int S = 0; S < SM; S++) u18[S] = ufrag(c, S, 18);
#pragma unroll
    for (int S = 0; S <= R - 3; S++) u26[S] = ufrag(c, S, 26);
    u10[0] = ufrag(c, R - 4, 10); u10[1] = ufrag(c, R - 3, 10);
    SB();
#pragma unroll
    for (int i = 0; i < 9; i++) a1 = mfb(c.F[8 - i], fu[8 + i], a1);
    u4 va[3], vb[3], vc[5], vd[3];
#pragma unroll
    for (int s = 0; s < 3; s++) { va[s] = utr(c, R - 2 + s, 18, 6); vb[s] = utr(c, R - 2 + s, 10, 14); }
    SB();
    u4 v2[3], v3[4];
#pragma unroll
    for (int s = 0; s < 3; s++) v2[s] = cat(cv4(mfb(u18[SM + s], c.B2b, z)), cv4(mfb(u18[SM + s], c.B2a, z)));
#pragma unroll
    for (int s = 0; s < 5; s++) vc[s] = wtr(c, s, 4, 12);
#pragma unroll
    for (int s = 0; s < 3; s++) vd[s] = wtr(c, 2 + s, 8, 16);
    SB();
#pragma unroll
    for (int S = 0; S <= R - 3; S++) v3[S] = cat(cv4(mfb(u18[S], c.B3, z)), cv4(mfb(u26[S], c.B4, z)));
    a0 = mfb(c.BF, u10[0], a0);
    a1 = mfb(c.BF, u10[1], a1);
    a0 = mfb(va[0], c.L1[1][0], a0); a1 = mfb(va[1], c.L1[1][0], a1);
    a0 = mfb(vb[0], c.L1[1][1], a0); a1 = mfb(vb[1], c.L1[1][1], a1);
    a0 = mfb(va[1], c.L1[0][0], a0); a1 = mfb(va[2], c.L1[0][0], a1);
    a0 = mfb(vb[1], c.L1[0][1], a0); a1 = mfb(vb[2], c.L1[0][1], a1);
    SB();
    a0 = mfb(v2[0], c.L2[1][1], a0); a1 = mfb(v2[1], c.L2[1][1], a1);
    a0 = mfb(v2[1], c.L2[0][1], a0); a1 = mfb(v2[2], c.L2[0][1], a1);
    a0 = mfb(vc[0], c.L2[1][0], a0); a1 = mfb(vc[1], c.L2[1][0], a1);
    a0 = mfb(vc[1], c.L2[0][0], a0); a1 = mfb(vc[2], c.L2[0][0], a1);
    a0 = mfb(vc[2], c.L1[1][2], a0); a1 = mfb(vc[3], c.L1[1][2], a1);
    a0 = mfb(vc[3], c.L1[0][2], a0); a1 = mfb(vc[4], c.L1[0][2], a1);
    a0 = mfb(vd[0], c.L1[1][3], a0); a1 = mfb(vd[1], c.L1[1][3], a1);
    a0 = mfb(vd[1], c.L1[0][3], a0); a1 = mfb(vd[2], c.L1[0][3], a1);
#pragma unroll
    for (int S = 0; S <= R - 3; S++) {
        if (R - S >= 4) a0 = mfb(v3[S], c.L3[R - S - 4], a0);
        a1 = mfb(v3[S], c.L3[R - S - 3], a1);
    }
    ldsw<u2>(c.st, cv4(a0));
    ldsw<u2>(c.st + 512, cv4(a1));
}

DEV void conv_near(const Ctx& c) {
    const f4 z = {0.f, 0.f, 0.f, 0.f};
    u4 u18[4], fu[33];
#pragma unroll
    for (int S = 0; S < 4; S++) u18[S] = ufrag(c, S, 18);
#pragma unroll
    for (int i = 0; i < 9; i++) fu[i] = ufrag(c, 0, 266 - 32 * i);
    SB();
    u4 v2;
    {
        f4 d[4];
#pragma unroll
        for (int S = 0; S < 4; S++) d[S] = mfb(c.B1, u18[S], z);
        v2 = cat(cv4(mfb(u18[0], c.B2b, z)), cv4(mfb(u18[0], c.B2a, z)));
#pragma unroll
        for (int S = 0; S < 4; S++) ldsw<u2>(c.ww + 512 * S, cv4(d[S]));
    }
#pragma unroll
    for (int i = 9; i < 17; i++) fu[i] = ufrag(c, 0, 266 - 32 * i);
    SB();
    f4 a0 = z, a1 = z, a2 = z, a3 = z;
#pragma unroll
    for (int i = 0; i < 9; i++) a0 = mfb(c.F[8 - i], fu[i], a0);
#pragma unroll
    for (int i = 17; i < 25; i++) fu[i] = ufrag(c, 0, 266 - 32 * i);
    SB();
#pragma unroll
    for (int i = 0; i < 9; i++) a1 = mfb(c.F[8 - i], fu[8 + i], a1);
    ldsw<u2>(c.st, cv4(a0));
#pragma unroll
    for (int i = 25; i < 33; i++) fu[i] = ufrag(c, 0, 266 - 32 * i);
    SB();
#pragma unroll
    for (int i = 0; i < 9; i++) a2 = mfb(c.F[8 - i], fu[16 + i], a2);
    u4 va[3], vb[3], vc[3], vd[3];
#pragma unroll
    for (int s = 0; s < 3; s++) { va[s] = utr(c, s, 18, 6); vb[s] = utr(c, s, 10, 14); }
    SB();
#pragma unroll
    for (int i = 0; i < 9; i++) a3 = mfb(c.F[8 - i], fu[24 + i], a3);
#pragma unroll
    for (int s = 0; s < 3; s++) { vc[s] = wtr(c, s, 4, 12); vd[s] = wtr(c, s, 8, 16); }
    SB();
    a1 = mfb(va[0], c.L1[0][0], a1); a2 = mfb(va[1], c.L1[0][0], a2); a3 = mfb(va[2], c.L1[0][0], a3);
    a1 = mfb(vb[0], c.L1[0][1], a1); a2 = mfb(vb[1], c.L1[0][1], a2); a3 = mfb(vb[2], c.L1[0][1], a3);
    a2 = mfb(va[0], c.L1[1][0], a2); a3 = mfb(va[1], c.L1[1][0], a3);
    a2 = mfb(vb[0], c.L1[1][1], a2); a3 = mfb(vb[1], c.L1[1][1], a3);
    a3 = mfb(v2, c.L2[0][1], a3);
    a1 = mfb(vc[0], c.L1[0][2], a1); a2 = mfb(vc[1], c.L1[0][2], a2); a3 = mfb(vc[2], c.L1[0][2], a3);
    a1 = mfb(vd[0], c.L1[0][3], a1); a2 = mfb(vd[1], c.L1[0][3], a2); a3 = mfb(vd[2], c.L1[0][3], a3);
    a2 = mfb(vc[0], c.L1[1][2], a2); a3 = mfb(vc[1], c.L1[1][2], a3);
    a2 = mfb(vd[0], c.L1[1][3], a2); a3 = mfb(vd[1], c.L1[1][3], a3);
    a3 = mfb(vc[0], c.L2[0][0], a3);
    ldsw<u2>(c.st + 512, cv4(a1));
    ldsw<u2>(c.st + 1024, cv4(a2));
    ldsw<u2>(c.st + 1536, cv4(a3));
}

__global__ __launch_bounds__(512, 2) void s4d_kernel(const unsigned short* __restrict__ u, const unsigned short* __restrict__ lar,
                                                     const unsigned short* __restrict__ aim, const unsigned short* __restrict__ cre,
                                                     const unsigned short* __restrict__ cim, const unsigned short* __restrict__ ldt,
                                                     const unsigned short* __restrict__ dsk, unsigned short* __restrict__ out
) {
    __shared__ __attribute__((aligned(16))) char smem[LDS_BYTES];
    const unsigned sb = (unsigned)(uintptr_t)(AS3 char*)smem;
    unsigned long long ts[16]; ts[11]=ts[12]=ts[13]=ts[14]=0;
    unsigned long long tc0 = __builtin_amdgcn_s_memtime();
    STAMP(0);
    const int tid = threadIdx.x;
    const int lane = tid & 63;
    const int w = __builtin_amdgcn_readfirstlane(tid >> 6);
    const int bx = blockIdx.x;
    const int xi = bx >> 3;
    const int part = xi / 10;
    const int grp = (bx & 7) * 10 + (xi - part * 10);
    const int ch = grp * 8 + w;
    const int nrows = 2048 - part * 512;

    const unsigned po = ch * NN;
    unsigned ldw = ((const unsigned*)ldt)[ch >> 1];
    unsigned dkw = ((const unsigned*)dsk)[ch >> 1];
    unsigned pl01 = *(const unsigned*)(lar + po + 2 * lane);
    unsigned pa01 = *(const unsigned*)(aim + po + 2 * lane);
    unsigned pr01 = *(const unsigned*)(cre + po + 2 * lane);
    unsigned pi01 = *(const unsigned*)(cim + po + 2 * lane);
    unsigned pr2 = (cre + po + 128)[lane];
    unsigned pi2 = (cim + po + 128)[lane];
    unsigned pl2 = (lar + po + 128)[lane];
    unsigned pa2 = (aim + po + 128)[lane];
    SB();
    unsigned pf;
    {
        auto rs = __builtin_amdgcn_make_buffer_rsrc((void*)u, (short)0, 2048 * 1280, 0x00020000);
        unsigned idx = xi * 137 + tid;
        unsigned off = (idx >> 1) * 1280 + (bx & 7) * 160 + (idx & 1) * 156;
        pf = __builtin_amdgcn_raw_buffer_load_b32(rs, tid < 137 ? off : 0x7fffff00u, 0, 0);
    }
    u4 ur[4];
    {
        auto rs = __builtin_amdgcn_make_buffer_rsrc((void*)u, (short)0, nrows * 1280, 0x00020000);
#pragma unroll
        for (int k = 0; k < 4; k++) ur[k] = __builtin_amdgcn_raw_buffer_load_b128(rs, tid * 5120 + k * 1280, grp * 16, 0);
    }
    SB();

    const unsigned tb = sb + w * T_STRIDE;
    const unsigned ub = sb + U_OFF + w * U_STRIDE;
    const unsigned fgb = sb + FG_OFF + w * FG_STRIDE;
    {
        const u4 z4 = {0u, 0u, 0u, 0u};
#pragma unroll
        for (int k = 0; k < 7; k++) ldsw<u4>(fgb + k * 1024 + lane * 16, z4);
        if (lane < 33) { ldsw<u4>(fgb + 7168 + lane * 16, z4); ldsw<u4>(ub + lane * 16, z4); }
        if (lane < 4) ldsw<unsigned>(lane == 3 ? ub + 528 : ub + 532 + 4096 + lane * 4, 0u);
    }
    const int la = lane & 15, lg = lane >> 4;
    Ctx c;
    c.B1 = band(la, 8 * lg, 1, 3, false);
    c.B2a = band(la, 8 * lg, 0, 5, false);
    c.B2b = band(la, 8 * lg, 8, 5, false);
    c.B3 = band(la, 8 * lg, 7, 7, false);
    c.B4 = band(la, 8 * lg, 6, 9, false);
    c.BF = band(la, 8 * lg, 6, 2, true);
    c.uf = opaque(ub + 32 * la + 16 * lg);
    c.ut = opaque(ub + 8 * lane);
    c.wt = opaque(tb + 8 * lane);
    c.ww = opaque(tb + 32 * la + 8 * lg);
    c.st = opaque(c.ww + (part == 2 ? 2048 : 3072));
    const unsigned fa = opaque(fgb + 706 * ((la + 3) & 3) - 2 * la + 16 * lg + 2);
    const unsigned ga = opaque(fgb + G_REL + 1218 * ((la + 1) & 3) - 2 * la + 8 * lg + 6);
    const unsigned tr = opaque(tb + 288 * la + 16 * lg);
    const unsigned wf = opaque(fgb + 2 * (28 + 16 * la + 4 * lg));
    const unsigned wg1 = opaque(fgb + G_REL + 2 * (112 * ((la & 7) >> 1) + 16 + 64 * (la & 1) - 16 * (la >> 3) + 4 * lg));
    const unsigned wg2 = opaque(fgb + G_REL + 2 * ((la < 4 ? 112 * la + 48 : 416 + 80 * (la >> 2) - 16 * (la & 3)) + 4 * lg));
    const unsigned wpt = opaque(fgb + (la < 12 ? 706 * (lane & 3) + 54 - 2 * (la >> 2) : G_REL + 1218 * (lane & 3) + 32));
    const float m63 = __uint_as_float(opaque(lane == 63 ? 0x3f800000u : 0u));
    const unsigned twa = opaque(tb + 4 * lane);
    const unsigned uw = opaque(sb + U_OFF + 532 + 8 * tid);
    asm volatile("s_mov_b32 m0, %0\n s_nop 0" :: "s"(tb) : "memory");
    STAMP(1);

    const float ldtf = __uint_as_float((ch & 1) ? (ldw & 0xffff0000u) : (ldw << 16));
    const float dskf = __uint_as_float((ch & 1) ? (dkw & 0xffff0000u) : (dkw << 16));
    const float LOG2E = 1.4426950408889634f;
    const float dt = __uint_as_float(pkbf(0.f, __builtin_amdgcn_exp2f(ldtf * LOG2E)) & 0xffff0000u);
    const float cm = -dt * LOG2E, cth = dt * 0.15915494309189535f;
    unsigned mx;
    {
        unsigned m0_ = max(pr01 & 0x7fff0000u, pi01 & 0x7fff0000u);
        unsigned m1_ = max(pr01 & 0x7fffu, pi01 & 0x7fffu);
        unsigned m2_ = max(pr2 & 0x7fffu, pi2 & 0x7fffu);
        mx = max(m0_, max(m1_, m2_) << 16);
        mx = max(mx, (unsigned)__builtin_amdgcn_update_dpp(0, (int)mx, 0x111, 0xf, 0xf, true));
        mx = max(mx, (unsigned)__builtin_amdgcn_update_dpp(0, (int)mx, 0x112, 0xf, 0xf, true));
        mx = max(mx, (unsigned)__builtin_amdgcn_update_dpp(0, (int)mx, 0x114, 0xf, 0xf, true));
        mx = max(mx, (unsigned)__builtin_amdgcn_update_dpp(0, (int)mx, 0x118, 0xf, 0xf, true));
        unsigned q0 = __builtin_amdgcn_readlane(mx, 15), q1 = __builtin_amdgcn_readlane(mx, 31);
        unsigned q2 = __builtin_amdgcn_readlane(mx, 47), q3 = __builtin_amdgcn_readlane(mx, 63);
        mx = max(max(q0, q1), max(q2, q3));
    }
    const unsigned ex = max(mx >> 23, 16u);
    const float sc = __uint_as_float((268u - ex) << 23);
    const float ks = dt * __uint_as_float((ex - 13u) << 23);
    STAMP(2);

    f4 acc0 = {0.f, 0.f, 0.f, 0.f}, acc1 = acc0, acc2 = acc0;
    unsigned cl = pl01 << 16, ca = pa01 << 16, cr = pr01 << 16, ci = pi01 << 16;
    unsigned nl = pl01 & 0xffff0000u, na = pa01 & 0xffff0000u, nr = pr01 & 0xffff0000u, ni = pi01 & 0xffff0000u;
#pragma unroll 1
    for (int gi = 0; gi < ITERS; gi++) {
        const float arp = __uint_as_float(pkbf(0.f, __builtin_amdgcn_exp2f(__uint_as_float(cl) * LOG2E)) & 0xffff0000u);
        const float m1 = arp * cm;
        const float tt = __uint_as_float(ca) * cth;
        const float th = __builtin_amdgcn_fractf(tt);
        const float th2 = __builtin_amdgcn_fractf(tt * 16.f);
        float mg2 = __builtin_amdgcn_exp2f(m1 * 16.f);
        float re2 = fmul(mg2, __builtin_amdgcn_cosf(th2)), im2 = fmul(mg2, __builtin_amdgcn_sinf(th2));
        const h2 x0 = pkh(fmul(__uint_as_float(cr), sc), fmul(__uint_as_float(ci), -sc));
        const h2 e16 = pkh(re2, im2); csq(re2, im2);
        const h2 e32 = pkh(re2, im2); csq(re2, im2);
        const h2 e64 = pkh(re2, im2); csq(re2, im2);
        const h2 e128 = pkh(re2, im2);
        SB();
        const h2 x1 = cmulc(x0, e16), x2 = cmulc(x0, e32); TW(15, x0); SB();
        const h2 x3 = cmulc(x1, e32), x4 = cmulc(x0, e64); TW(14, x1); TW(13, x2); SB();
        const h2 x5 = cmulc(x1, e64), x6 = cmulc(x2, e64); TW(12, x3); TW(11, x4); SB();
        const h2 x7 = cmulc(x3, e64); h2 t0 = cmulc(x0, e128); TW(10, x5); TW(9, x6); SB();
        h2 t1 = cmulc(x1, e128), t2 = cmulc(x2, e128); TW(8, x7); TW(7, t0); SB();
        h2 t3 = cmulc(x3, e128); t0 = cmulc(x4, e128); TW(6, t1); TW(5, t2); SB();
        t1 = cmulc(x5, e128); t2 = cmulc(x6, e128); TW(4, t3); TW(3, t0); SB();
        t3 = cmulc(x7, e128);
        float mg = __builtin_amdgcn_exp2f(m1);
        float re = fmul(mg, __builtin_amdgcn_cosf(th)), im = fmul(mg, __builtin_amdgcn_sinf(th));
        TW(2, t1); TW(1, t2); SB();
        TW(0, t3);
        const h2 e1 = pkh(re, im); csq(re, im);
        u4 xf[4];
#pragma unroll
        for (int s = 0; s < 4; s++) xf[s] = ldsr<u4>(tr + 64 * s);
        const h2 e2 = pkh(re, im); csq(re, im);
        SB();
        const h2 one = {(_Float16)1.f, (_Float16)0.f};
        const h2 e4 = pkh(re, im); csq(re, im);
        TW(15, one); TW(14, e1); SB();
        const h2 e8 = pkh(re, im);
        t0 = cmul(e2, e1); t1 = cmul(e4, e1); TW(13, e2); TW(11, e4); SB();
        const h2 y6 = cmul(e4, e2); t2 = cmul(e8, e1); TW(12, t0); TW(10, t1); TW(7, e8); SB();
        const h2 y10 = cmul(e8, e2), y12 = cmul(e8, e4); TW(9, y6); TW(6, t2); SB();
        t0 = cmul(y6, e1); const h2 y14 = cmul(e8, y6); TW(5, y10); TW(3, y12); SB();
        t1 = cmul(y10, e1); t2 = cmul(y12, e1); TW(8, t0); TW(1, y14); SB();
        t3 = cmul(y14, e1); csq(re2, im2); TW(4, t1); TW(2, t2); SB();
        TW(0, t3);
        const h2 e256 = pkh(re2, im2); csq(re2, im2);
        u4 yf[4];
#pragma unroll
        for (int s = 0; s < 4; s++) yf[s] = ldsr<u4>(tr + 64 * s);
        const h2 e512 = pkh(re2, im2); csq(re2, im2);
        const h2 p272 = cmul(e256, e16); t0 = cmul(e256, e4);
        SB();
        t1 = cmul(e256, y6); const h2 p264 = cmul(e256, e8); TW(7, p272); TW(1, t0); SB();
        t2 = cmul(e256, y10); t3 = cmul(e256, y12); TW(2, t1); TW(3, p264); SB();
        acc0 = mfh(yf[0], xf[0], acc0);
        t0 = cmul(e256, y14); t1 = cmul(p272, e2); TW(4, t2); TW(5, t3); SB();
        acc0 = mfh(yf[1], xf[1], acc0);
        const h2 p520 = cmul(e512, e8), p528 = cmul(e512, e16); TW(6, t0); TW(0, t1); SB();
        acc0 = mfh(yf[2], xf[2], acc0);
        const h2 p516 = cmul(e512, e4), p524 = cmul(e512, y12); TW(10, p520); TW(11, p520); TW(12, p520); SB();
        acc0 = mfh(yf[3], xf[3], acc0);
        const h2 e1024 = pkh(re2, im2);
        const h2 p776 = cmul(e256, p520); TW(8, p528); TW(14, p528); TW(15, p528); SB();
        const h2 p784 = cmul(e256, p528); t0 = cmul(e256, p516); TW(9, p516); TW(13, p524);
#pragma unroll
        for (int s = 0; s < 4; s++) yf[s] = ldsr<u4>(tr + 64 * s);
        SB();
        t1 = cmul(e256, p524); t2 = cmul(e1024, e8); TW(1, p776); TW(3, p784); TW(0, t0); SB();
        t3 = cmul(e1024, p264); t0 = cmul(e1024, p520); TW(2, t1); TW(4, t2); SB();
        acc1 = mfh(xf[0], yf[0], acc1);
        t1 = cmul(e1024, p776); t2 = cmul(e1024, e16); TW(5, t3); TW(6, t0); SB();
        acc1 = mfh(xf[1], yf[1], acc1);
        t3 = cmul(e1024, p272); t0 = cmul(e1024, p528); TW(7, t1); TW(8, t2); SB();
        acc1 = mfh(xf[2], yf[2], acc1);
        t1 = cmul(e1024, p784); t2 = cmul(e256, e2); TW(9, t3); TW(10, t0); TW(12, e256); SB();
        acc1 = mfh(xf[3], yf[3], acc1);
        TW(11, t1); TW(13, t2);
#pragma unroll
        for (int s = 0; s < 4; s++) yf[s] = ldsr<u4>(tr + 64 * s);
        cl = nl; ca = na; cr = nr; ci = ni;
        nl = pl2 << 16; na = pa2 << 16; nr = pr2 << 16; ni = pi2 << 16;
#pragma unroll
        for (int s = 0; s < 4; s++) acc2 = mfh(xf[s], yf[s], acc2);
    }
    STAMP(3);
    {
        float k3 = __builtin_fmaf(m63, dskf, fmul(acc0[3], ks));
        put4(wf, 704, pkbf(fmul(acc0[0], ks), fmul(acc0[1], ks)), pkbf(fmul(acc0[2], ks), k3));
        unsigned r01 = pkbf(fmul(acc1[0], ks), fmul(acc1[1], ks));
        put4(wg1, 1216, r01, pkbf(fmul(acc1[2], ks), fmul(acc1[3], ks)));
        unsigned k512 = __builtin_amdgcn_readlane(r01, 7);
        unsigned q01 = pkbf(fmul(acc2[0], ks), fmul(acc2[1], ks)), q23 = pkbf(fmul(acc2[2], ks), fmul(acc2[3], ks));
        if (la < 12) put4(wg2, 1216, q01, q23);
        unsigned k256 = __builtin_amdgcn_readlane(q23, 60) >> 16;
        unsigned k258 = __builtin_amdgcn_readlane(q23, 61) >> 16;
        unsigned k1024 = __builtin_amdgcn_readlane(q01, 3) & 0xffffu;
        asm volatile("" ::: "memory");
        if (lane < 16) ldsw<unsigned short>(wpt, (unsigned short)(lane < 8 ? k256 : (lane < 12 ? k258 : k512)));
#pragma unroll
        for (int m = 0; m < 4; m++) c.BF[m] = __umul24(c.BF[m], k1024);
    }
    asm volatile("" ::: "memory");
#pragma unroll
    for (int i = 0; i < 9; i++) c.F[i] = cat(ldsrv<u2>(fa + 544 - 64 * i), ldsrv<u2>(fa + 552 - 64 * i));
#pragma unroll
    for (int d = 0; d < 2; d++) {
        c.L1[d][0] = gfrag(ga, YT(18), YT(6), d + 1);
        c.L1[d][1] = gfrag(ga, YT(10), YT(14), d + 1);
        c.L1[d][2] = gfrag(ga, YT(4), YT(12), d + 1);
        c.L1[d][3] = gfrag(ga, YT(8), YT(16), d + 1);
        c.L2[d][0] = gfrag(ga, YT(4), YT(12), d + 3);
        c.L2[d][1] = gfrag(ga, YT(8), YT(16), d + 3);
    }
#pragma unroll
    for (int d = 0; d < 4; d++) c.L3[d] = gfrag(ga, YH8, YH16, d + 4);
    STAMP(4);
    asm volatile("" :: "v"(pf));
#pragma unroll
    for (int d = 0; d < 4; d++) {
        unsigned lo01 = __builtin_amdgcn_perm(ur[1][d], ur[0][d], 0x05040100u);
        unsigned lo23 = __builtin_amdgcn_perm(ur[3][d], ur[2][d], 0x05040100u);
        unsigned hi01 = __builtin_amdgcn_perm(ur[1][d], ur[0][d], 0x07060302u);
        unsigned hi23 = __builtin_amdgcn_perm(ur[3][d], ur[2][d], 0x07060302u);
        ldsw<unsigned>(uw + (2 * d) * U_STRIDE, lo01);
        ldsw<unsigned>(uw + (2 * d) * U_STRIDE + 4, lo23);
        ldsw<unsigned>(uw + (2 * d + 1) * U_STRIDE, hi01);
        ldsw<unsigned>(uw + (2 * d + 1) * U_STRIDE + 4, hi23);
    }
    STAMP(5);
    __syncthreads();
    STAMP(6);
    if (part == 0) conv_far<6>(c);
    else if (part == 1) conv_far<4>(c);
    else conv_near(c);
    STAMP(7);
    __syncthreads();
    STAMP(8);
    {
        const unsigned ea = sb + ((lane >> 2) & 3) * T_STRIDE + (part == 2 ? 2048 : 3072) + 2 * (64 * w + 16 * lg + 4 * (lane & 3));
        const int row0 = part == 2 ? 0 : nrows - 512;
        char* op = (char*)out + (size_t)(row0 + tid) * 1280 + grp * 16;
        u4 v = cat(ldtr(ea), ldtr(ea + 4 * T_STRIDE));
        *(u4*)op = v;
        if (part == 2) {
            u4 v2 = cat(ldtr(ea + 1024), ldtr(ea + 1024 + 4 * T_STRIDE));
            *(u4*)(op + 512 * 1280) = v2;
        }
    }
    STAMP(9);
    asm volatile("s_waitcnt vmcnt(0)" ::: "memory");
    STAMP(10);
    { unsigned long long tgt = ts[0] + 3000 + 4 * (ts[TSPIN] - ts[0]);
      if ((blockIdx.x & 7) == 1) while (__builtin_amdgcn_s_memrealtime() < tgt) __builtin_amdgcn_s_sleep(2); }
}

extern "C" void kernel_launch(void* const* d_in, const int* in_sizes, int n_in, void* d_out, int out_size, void* d_ws, size_t ws_size,
                              hipStream_t stream) {
    s4d_kernel<<<240, 512, 0, stream>>>((const unsigned short*)d_in[0], (const unsigned short*)d_in[1], (const unsigned short*)d_in[2],
                                        (const unsigned short*)d_in[3], (const unsigned short*)d_in[4], (const unsigned short*)d_in[5],
                                        (const unsigned short*)d_in[6], (unsigned short*)d_out
    );
}
```

```cpp
#include <hip/hip_runtime.h>
#include <stdint.h>

typedef _Float16 h2 __attribute__((ext_vector_type(2)));
typedef _Float16 h8 __attribute__((ext_vector_type(8)));
typedef __bf16 b2 __attribute__((ext_vector_type(2)));
typedef __bf16 b8 __attribute__((ext_vector_type(8)));
typedef float f2 __attribute__((ext_vector_type(2)));
typedef float f4 __attribute__((ext_vector_type(4)));
typedef unsigned u2 __attribute__((ext_vector_type(2)));
typedef unsigned u4 __attribute__((ext_vector_type(4)));
typedef unsigned long long l8 __attribute__((ext_vector_type(8)));
typedef short s4 __attribute__((ext_vector_type(4)));
#define AS3 __attribute__((address_space(3)))
#define DEV __device__ __forceinline__
#define SB() __builtin_amdgcn_sched_barrier(0)
#define CFENCE() asm volatile("" ::: "memory")

#define NN 192
#define CODE_BYTES 14400

#define T_STRIDE 4608
#define U_OFF 55296
#define U_STRIDE 4640
#define W_OFF 73856
#define W_STRIDE 3648
#define VF_OFF 88448
#define VF_STRIDE 9216
#define FG_OFF 125312
#define CS 1856
#define FG_STRIDE (4 * CS)
#define F_REL 1232
#define MISC_OFF (FG_OFF + 4 * FG_STRIDE)
#define P_OFF (MISC_OFF + 64)
#define LDS_BYTES (P_OFF + 8192)
#define S_C0 2u
#define S_C1 8u
#define S_C2 32u
#define S_F 64u
#define S_G1 128u
#define S_G2 256u
#define S_W01 512u
#define S_W23 1024u
#define S_W45 2048u
#define S_U(h) (4u << (12 + 3 * (h)))

#define STAMP(i)

template <class T> DEV T ldsr(unsigned a) { return *(const AS3 T*)(uintptr_t)(a); }
template <class T> DEV T ldsrv(unsigned a) { return *(const volatile AS3 T*)(uintptr_t)(a); }
template <class T> DEV void ldsw(unsigned a, T v) { *(AS3 T*)(uintptr_t)(a) = v; }
DEV u2 ldtr(unsigned a) { s4 v = __builtin_amdgcn_ds_read_tr16_b64_v4i16((AS3 s4*)(uintptr_t)(a)); return __builtin_bit_cast(u2, v); }
DEV unsigned opaque(unsigned x) { asm volatile("" : "+v"(x)); return x; }

DEV unsigned pkbf(float a, float b) { f2 v = {a, b}; return __builtin_bit_cast(unsigned, __builtin_convertvector(v, b2)); }
DEV h2 pkh(float a, float b) { f2 v = {a, b}; return __builtin_convertvector(v, h2); }
DEV h2 cmul(h2 h, h2 e) { h2 t = h * (h2){e.x, e.x}; return __builtin_elementwise_fma((h2){-h.y, h.x}, (h2){e.y, e.y}, t); }
DEV h2 cmulc(h2 h, h2 e) { h2 t = h * (h2){e.x, e.x}; return __builtin_elementwise_fma((h2){h.y, -h.x}, (h2){e.y, e.y}, t); }
DEV f4 mfb(u4 a, u4 b, f4 c) { return __builtin_amdgcn_mfma_f32_16x16x32_bf16(__builtin_bit_cast(b8, a), __builtin_bit_cast(b8, b), c, 0, 0, 0); }
DEV f4 mfh(u4 a, u4 b, f4 c) { return __builtin_amdgcn_mfma_f32_16x16x32_f16(__builtin_bit_cast(h8, a), __builtin_bit_cast(h8, b), c, 0, 0, 0); }
DEV u4 cat(u2 a, u2 b) { return (u4){a.x, a.y, b.x, b.y}; }

#define TW(row, val) asm volatile("ds_write_addtid_b32 %0 offset:%1" :: "v"(val), "i"((row) * 288) : "memory")

DEV void csq(float& re, float& im) { float t = im * im; float d = re + re; im = d * im; re = __builtin_fmaf(re, re, -t); }

DEV void arrive(unsigned a, unsigned v) {
    asm volatile("s_mov_b64 exec, 1\n ds_add_u32 %0, %1\n s_mov_b64 exec, -1" :: "v"(a), "v"(v) : "memory");
}
DEV void arrive4(unsigned a, unsigned v) {
    asm volatile("s_mov_b64 exec, 15\n ds_add_u32 %0, %1\n s_mov_b64 exec, -1" :: "v"(a), "v"(v) : "memory");
}
DEV void waitm(unsigned a, unsigned m) {
    CFENCE();
    while ((__builtin_amdgcn_readfirstlane(ldsrv<unsigned>(a)) & m) != m) {}
    CFENCE();
}

DEV u4 band(int b, int g8, int lo, int len, bool sel) {
    unsigned by = ((((1u << len) - 1u) << lo) << b >> g8) & 0xffu;
    unsigned e = by | (by << 15);
    u4 r;
#pragma unroll
    for (int m = 0; m < 4; m++) r[m] = opaque(sel ? ((e >> (2 * m)) & 0x10001u) : __umul24(e & (0x10001u << (2 * m)), 0x3F80u >> (2 * m)));
    return r;
}

DEV void put4(unsigned a, unsigned p01, unsigned p23) {
    unsigned p12 = __builtin_amdgcn_alignbit(p23, p01, 16);
    ldsw<u2>(a, (u2){p01, p23});
    ldsw<unsigned short>(a + CS + 2, (unsigned short)p01);
    ldsw<unsigned>(a + CS + 4, p12);
    ldsw<unsigned short>(a + CS + 8, (unsigned short)(p23 >> 16));
    ldsw<unsigned>(a + 2 * CS + 4, p01);
    ldsw<unsigned>(a + 2 * CS + 8, p23);
    ldsw<unsigned short>(a + 3 * CS + 6, (unsigned short)p01);
    ldsw<unsigned>(a + 3 * CS + 8, p12);
    ldsw<unsigned short>(a + 3 * CS + 12, (unsigned short)(p23 >> 16));
}

struct Ctx {
    unsigned uf;
    unsigned ut;
    unsigned wt;
    unsigned vf;
    unsigned fa, ga;
    unsigned st;
    unsigned sw;
    u4 BF;
};

DEV u4 ufrag(unsigned uf, int S, int s0) { return ldsr<u4>(uf + 512 * S + 2 * (266 - s0)); }
DEV u2 gq(unsigned base, int ytop, int dl) { return ldsrv<u2>(base + 2 * (ytop - 3 - 16 * dl)); }
#define PP(ph) ((ph) == 18 ? 0 : (ph) / 2 - 1)
#define YT(ph) (112 * (PP(ph) >> 1) + ((PP(ph) & 1) ? 111 : 47))
#define YH8 (448 + 127)
#define YH16 (528 + 127)
DEV u4 gfrag(unsigned ga, int y1, int y2, int dl) { return cat(gq(ga, y1, dl), gq(ga, y2, dl)); }
DEV u4 ffrag(unsigned fa, int i) { return cat(ldsrv<u2>(fa + 544 - 64 * i), ldsrv<u2>(fa + 552 - 64 * i)); }
DEV u2 cv4(f4 d) { return (u2){pkbf(d[0], d[1]), pkbf(d[2], d[3])}; }
DEV u4 utr(const Ctx& c, int S, int p1, int p2) { return cat(ldtr(c.ut + 512 * S + 2 * (266 - p1)), ldtr(c.ut + 512 * S + 2 * (266 - p2))); }
DEV u4 wtr(const Ctx& c, int S, int p1, int p2) { return cat(ldtr(c.wt + 512 * S + 2 * (16 - p1)), ldtr(c.wt + 512 * S + 2 * (16 - p2))); }

template <int R> DEV void conv_pair(const Ctx& c) {
    const f4 z = {0.f, 0.f, 0.f, 0.f};
    f4 a0 = z, a1 = z;
    u4 F[9], fu[17];
#pragma unroll
    for (int i = 0; i < 3; i++) { F[i] = ffrag(c.fa, 8 - i); fu[i] = ufrag(c.uf, R, 266 - 32 * i); fu[8 + i] = ufrag(c.uf, R, 10 - 32 * i); }
    SB();
#pragma unroll
    for (int i = 3; i < 6; i++) { F[i] = ffrag(c.fa, 8 - i); fu[i] = ufrag(c.uf, R, 266 - 32 * i); fu[8 + i] = ufrag(c.uf, R, 10 - 32 * i); }
#pragma unroll
    for (int i = 0; i < 3; i++) { a0 = mfb(F[i], fu[i], a0); a1 = mfb(F[i], fu[8 + i], a1); }
    SB();
#pragma unroll
    for (int i = 6; i < 9; i++) { F[i] = ffrag(c.fa, 8 - i); if (i < 8) fu[i] = ufrag(c.uf, R, 266 - 32 * i); fu[8 + i] = ufrag(c.uf, R, 10 - 32 * i); }
#pragma unroll
    for (int i = 3; i < 6; i++) { a0 = mfb(F[i], fu[i], a0); a1 = mfb(F[i], fu[8 + i], a1); }
    SB();
    u4 va[3], vb[3], ka[2], kb[2];
#pragma unroll
    for (int s = 0; s < 3; s++) if (R - 2 + s >= 0) { va[s] = utr(c, R - 2 + s, 18, 6); vb[s] = utr(c, R - 2 + s, 10, 14); }
#pragma unroll
    for (int d = 0; d < 2; d++) if (R > 0 || d == 0) { ka[d] = gfrag(c.ga, YT(18), YT(6), d + 1); kb[d] = gfrag(c.ga, YT(10), YT(14), d + 1); }
#pragma unroll
    for (int i = 6; i < 9; i++) { a0 = mfb(F[i], fu[i], a0); a1 = mfb(F[i], fu[8 + i], a1); }
    SB();
    u4 vc[5], vd[3], kc[4], kd[4];
#pragma unroll
    for (int s = 0; s < 3; s++) if (R - 2 + s >= 0) { vc[2 + s] = wtr(c, R - 2 + s, 4, 12); vd[s] = wtr(c, R - 2 + s, 8, 16); }
#pragma unroll
    for (int d = 0; d < 2; d++) if (R > 0 || d == 0) { kc[d] = gfrag(c.ga, YT(4), YT(12), d + 1); kd[d] = gfrag(c.ga, YT(8), YT(16), d + 1); }
    a1 = mfb(va[2], ka[0], a1); a1 = mfb(vb[2], kb[0], a1);
    if (R > 0) {
        a0 = mfb(va[1], ka[0], a0); a0 = mfb(vb[1], kb[0], a0);
        a0 = mfb(va[0], ka[1], a0); a1 = mfb(va[1], ka[1], a1);
        a0 = mfb(vb[0], kb[1], a0); a1 = mfb(vb[1], kb[1], a1);
    }
    SB();
    u4 v2[3];
    if (R == 2) waitm(c.sw, S_G2);
    if (R > 0) {
#pragma unroll
        for (int s = 0; s < 3; s++) if (R - 4 + s >= 0) { if (s < 2) vc[s] = wtr(c, R - 4 + s, 4, 12); v2[s] = ldsr<u4>(c.vf + 1024 * (R - 4 + s)); }
#pragma unroll
        for (int d = 2; d < 4; d++) if (R > 2 || d == 2) { kc[d] = gfrag(c.ga, YT(4), YT(12), d + 1); kd[d] = gfrag(c.ga, YT(8), YT(16), d + 1); }
    }
    a1 = mfb(vc[4], kc[0], a1); a1 = mfb(vd[2], kd[0], a1);
    if (R > 0) {
        a0 = mfb(vc[3], kc[0], a0); a0 = mfb(vd[1], kd[0], a0);
        a0 = mfb(vc[2], kc[1], a0); a1 = mfb(vc[3], kc[1], a1);
        a0 = mfb(vd[0], kd[1], a0); a1 = mfb(vd[1], kd[1], a1);
    }
    SB();
    u4 v3[4], kh[4], u10[2];
    if (R >= 4) {
#pragma unroll
        for (int S = 0; S <= R - 3; S++) { v3[S] = ldsr<u4>(c.vf + 1024 * (5 + S)); kh[S] = gfrag(c.ga, YH8, YH16, 4 + S); }
        u10[0] = ufrag(c.uf, R - 4, 10); u10[1] = ufrag(c.uf, R - 3, 10);
    }
    if (R > 0) {
        a1 = mfb(vc[2], kc[2], a1); a1 = mfb(v2[2], kd[2], a1);
        if (R > 2) {
            a0 = mfb(vc[1], kc[2], a0); a0 = mfb(v2[1], kd[2], a0);
            a0 = mfb(vc[0], kc[3], a0); a1 = mfb(vc[1], kc[3], a1);
            a0 = mfb(v2[0], kd[3], a0); a1 = mfb(v2[1], kd[3], a1);
        }
    }
    SB();
    if (R >= 4) {
        a0 = mfb(c.BF, u10[0], a0);
        a1 = mfb(c.BF, u10[1], a1);
#pragma unroll
        for (int S = 0; S <= R - 3; S++) {
            if (R - S >= 4) a0 = mfb(v3[S], kh[R - S - 4], a0);
            a1 = mfb(v3[S], kh[R - S - 3], a1);
        }
    }
    ldsw<u2>(c.st, cv4(a0));
    ldsw<u2>(c.st + 512, cv4(a1));
}

struct Bands { u4 B1, B2a, B2b, B3, B4; };
template <int S> DEV void build(const Ctx& c, const Bands& q, unsigned ww, int la) {
    const f4 z = {0.f, 0.f, 0.f, 0.f};
    u4 u18[2], u26[2];
#pragma unroll
    for (int s = 0; s < 2; s++) { u18[s] = ufrag(c.uf, S + s, 18); if (S < 4) u26[s] = ufrag(c.uf, S + s, 26); }
    ldsw<u2>(ww + 512 * S, cv4(mfb(q.B1, u18[0], z)));
    u2 w1 = cv4(mfb(q.B1, u18[1], z));
    if (S < 6 || la == 0) ldsw<u2>(ww + 512 * (S + 1), w1);
#pragma unroll
    for (int s = 0; s < 2; s++) {
        if (S + s < 5) ldsw<u4>(c.vf + 1024 * (S + s), cat(cv4(mfb(u18[s], q.B2b, z)), cv4(mfb(u18[s], q.B2a, z))));
        if (S < 4) ldsw<u4>(c.vf + 1024 * (5 + S + s), cat(cv4(mfb(u18[s], q.B3, z)), cv4(mfb(u26[s], q.B4, z))));
    }
}

#define PROF_OUT()
#define KILL(x) asm volatile("" : "=v"(x))
#define KILLALL() do { KILL(q.B1); KILL(q.B2a); KILL(q.B2b); KILL(q.B3); KILL(q.B4); KILL(acc0); KILL(acc1); KILL(acc2); KILL(wk); KILL(pw); KILL(pe); \
    KILL(dskf); KILL(ww); } while (0)
#define EPILOGUE(rr) do { \
    STAMP(9); \
    unsigned na; \
    asm volatile("s_mov_b64 exec, 1\n ds_add_rtn_u32 %0, %1, %2\n s_mov_b64 exec, -1\n s_waitcnt lgkmcnt(0)" \
                 : "=v"(na) : "v"(sb + MISC_OFF + 16 + 4 * (rr)), "v"(1u) : "memory"); \
    STAMP(10); \
    if (__builtin_amdgcn_readfirstlane(na) == 3) { \
        __builtin_amdgcn_s_setprio(3); \
        const unsigned ea = sb + ((rr) == 3 ? 8 * T_STRIDE : 4 * (rr) * T_STRIDE + 2048) + ((lane >> 2) & 3) * (T_STRIDE + 64) + 2 * (16 * lg + 4 * (lane & 3)); \
        auto ro = __builtin_amdgcn_make_buffer_rsrc((void*)ka[7], (short)0, 2048 * 1280, 0x00020000); \
        const unsigned vo = lane * 1280 + grp * 8; \
        u2 v[8]; \
        _Pragma("unroll") for (int k = 0; k < 8; k++) v[k] = ldtr(ea + 128 * k); \
        _Pragma("unroll") for (int k = 0; k < 8; k++) __builtin_amdgcn_raw_buffer_store_b64(v[k], ro, vo, (512 * (rr) + 64 * k) * 1280, 0); \
    } \
    PROF_OUT(); \
    asm volatile("s_endpgm" ::: "memory"); \
} while (0)

__global__ __launch_bounds__(1024, 4) void s4d_kernel(const unsigned short* __restrict__ u, const unsigned short* __restrict__ lar,
                                                      const unsigned short* __restrict__ aim, const unsigned short* __restrict__ cre,
                                                      const unsigned short* __restrict__ cim, const unsigned short* __restrict__ ldt,
                                                      const unsigned short* __restrict__ dsk, unsigned short* __restrict__ out
) {
    __shared__ __attribute__((aligned(16))) char smem[LDS_BYTES];
    const unsigned sb = (unsigned)(uintptr_t)(AS3 char*)smem;
    STAMP(0);
    const int tid = threadIdx.x;
    l8 ka;
    unsigned cpf;
    {
        unsigned long long pc;
        asm volatile("s_load_dwordx16 %0, %2, 0x0\n"
                     "s_getpc_b64 %1" : "=&s"(ka), "=&s"(pc) : "s"(__builtin_amdgcn_kernarg_segment_ptr()));
        asm volatile("v_lshlrev_b32 %0, 7, %2\n"
                     "v_min_u32 %0, %3, %0\n"
                     "global_load_dword %0, %0, %1"
                     : "=&v"(cpf) : "s"(pc), "v"(tid), "i"(CODE_BYTES));
    }
    SB();
    const int lane = tid & 63;
    const int w = __builtin_amdgcn_readfirstlane(tid >> 6);
    const int c4 = w & 3, r = w >> 2;
    const int bx = blockIdx.x;
    const int xi = bx >> 3;
    const int grp = (bx & 7) * 20 + xi;
    const int ch = grp * 4 + c4;
    const int la = lane & 15, lg = lane >> 4;

    const unsigned ub = sb + U_OFF + c4 * U_STRIDE;
    const unsigned fgb = sb + FG_OFF + c4 * FG_STRIDE;
    const u4 z4 = {0u, 0u, 0u, 0u};
    Ctx c;
    Bands q;
    unsigned ww;
#define COMMON_SETUP() \
    c.uf = opaque(ub + 32 * la + 16 * lg); \
    c.ut = opaque(ub + 8 * lane); \
    c.wt = opaque(sb + W_OFF + c4 * W_STRIDE + 8 * lane); \
    c.vf = opaque(sb + VF_OFF + c4 * VF_STRIDE + 16 * lane); \
    c.st = opaque(sb + (r == 3 ? 8 * T_STRIDE : 4 * r * T_STRIDE + 2048) + c4 * (T_STRIDE + 64) + 32 * la + 8 * lg); \
    c.fa = opaque(fgb + F_REL + (CS + 2) * ((la + 3) & 3) - 2 * la + 16 * lg + 2); \
    c.ga = opaque(fgb + (CS + 2) * ((la + 1) & 3) - 2 * la + 8 * lg + 6); \
    c.sw = opaque(sb + MISC_OFF + 4 * c4); \
    ww = opaque(sb + W_OFF + c4 * W_STRIDE + 32 * la + 8 * lg); \
    q.B1 = band(la, 8 * lg, 1, 3, false); \
    q.B2a = band(la, 8 * lg, 0, 5, false); \
    q.B2b = band(la, 8 * lg, 8, 5, false);
    unsigned pl, pa, pr, pi, ldh, dkh, poff, uo;
    {
        ldsw<u4>(sb + FG_OFF + tid * 16, z4);
        const unsigned zq = tid - 836, zj = min(zq >> 2, 37u);
        const unsigned za = U_OFF + (zq & 3) * U_STRIDE + (zj < 34 ? zj * 16 : zj * 1024 + (528 - 33 * 1024));
        ldsw<u4>(sb + (tid < 836 ? FG_OFF + 16384 + tid * 16 : za), z4);
        const unsigned vc = opaque(2 * ch), vo = opaque(2 * (ch * NN + min(r * 64 + lane, NN - 1)));
        const int lt = tid & 255;
        const unsigned idx = min(lt * 20 + xi, 4095);
        poff = opaque((idx >> 1) * 1280 + (bx & 7) * 160 + (idx & 1) * 156);
        uo = opaque(lt * 2560 + grp * 8);
        asm volatile("s_waitcnt lgkmcnt(0)" :: "s"(ka) : "memory");
        asm volatile("global_load_ushort %0, %1, %2" : "=v"(ldh) : "v"(vc), "s"(ka[5]));
        asm volatile("global_load_ushort %0, %1, %2" : "=v"(pl) : "v"(vo), "s"(ka[1]));
        asm volatile("global_load_ushort %0, %1, %2" : "=v"(pa) : "v"(vo), "s"(ka[2]));
        asm volatile("global_load_ushort %0, %1, %2" : "=v"(pr) : "v"(vo), "s"(ka[3]));
        asm volatile("global_load_ushort %0, %1, %2" : "=v"(pi) : "v"(vo), "s"(ka[4]));
        asm volatile("global_load_ushort %0, %1, %2" : "=v"(dkh) : "v"(vc), "s"(ka[6]));
        STAMP(1);
        asm volatile("s_barrier" ::: "memory");
        STAMP(2);
        SB();
    }
    if (r == 3) {
        __builtin_amdgcn_s_setprio(3);
        unsigned pf;
        u2 ua[2], uc[2];
#define ULOAD(x) \
        asm volatile("global_load_dwordx2 %0, %1, %2" : "=v"(x[0]) : "v"(uo), "s"(ka[0])); \
        asm volatile("global_load_dwordx2 %0, %1, %2 offset:1280" : "=v"(x[1]) : "v"(uo), "s"(ka[0])); \
        uo += 512 * 1280;
#define UW(cc, h, val) asm volatile("ds_write_addtid_b32 %0 offset:%1" :: "v"(val), "i"((cc) * U_STRIDE + 1024 * (h)) : "memory")
#define UPUT(x, h, n) \
        asm volatile("s_waitcnt vmcnt(" #n ")" : "+v"(x[0]), "+v"(x[1])); \
        UW(0, h, __builtin_amdgcn_perm(x[1][0], x[0][0], 0x05040100u)); \
        UW(1, h, __builtin_amdgcn_perm(x[1][0], x[0][0], 0x07060302u)); \
        UW(2, h, __builtin_amdgcn_perm(x[1][1], x[0][1], 0x05040100u)); \
        UW(3, h, __builtin_amdgcn_perm(x[1][1], x[0][1], 0x07060302u)); \
        arrive4(sw4, 1u << (12 + 3 * (h))); SB();
        asm volatile("global_load_dword %0, %1, %2" : "=v"(pf) : "v"(poff), "s"(ka[0]));
        ULOAD(ua); ULOAD(uc);
        SB();
        STAMP(3);
        COMMON_SETUP();
        c.BF = band(la, 8 * lg, 6, 2, true);
        const unsigned sw4 = opaque(sb + MISC_OFF + 4 * lane);
        asm volatile("s_mov_b32 m0, %0\n s_nop 0" :: "s"(sb + U_OFF + 532 + 256 * c4) : "memory");
        SB();
        UPUT(ua, 0, 2);
        asm volatile("" :: "v"(pf), "v"(cpf), "v"(pl), "v"(pa), "v"(pr), "v"(pi), "v"(ldh), "v"(dkh));
        STAMP(4);
        ULOAD(ua); SB();
        UPUT(uc, 1, 2);
        ULOAD(uc); SB();
        UPUT(ua, 2, 2);
        STAMP(5);
        waitm(c.sw, S_U(2));
        build<4>(c, q, ww, la);
        arrive(c.sw, S_W45);
        SB();
        UPUT(uc, 3, 0);
        STAMP(6);
        waitm(c.sw, S_U(3));
        build<6>(c, q, ww, la);
        __builtin_amdgcn_s_setprio(0);
        STAMP(7);
        waitm(c.sw, S_F | S_G1 | S_G2 | S_W01 | S_W23);
        STAMP(8);
        {
            unsigned k1024 = ldsr<unsigned>(c.sw + 32);
#pragma unroll
            for (int m = 0; m < 4; m++) c.BF[m] = __umul24(c.BF[m], k1024);
        }
        conv_pair<6>(c);
        EPILOGUE(3);
        KILL(cpf); KILL(pl); KILL(pa); KILL(pr); KILL(pi); KILL(ldh); KILL(dkh);
    }
    {
        __builtin_amdgcn_s_setprio(2);
        const unsigned tb = sb + w * T_STRIDE;
        const unsigned tr = opaque(tb + 288 * la + 16 * lg);
        unsigned wk;
        {
            const int p1 = la < 8 ? la : (int)((0x14325760u >> (4 * (la - 8))) & 7u);
            const int y0 = F_REL / 2 + 28 + 16 * la;
            const int y1 = 112 * (p1 >> 1) + 16 + 64 * (p1 & 1) - 16 * (la >> 3);
            const int y2 = la < 4 ? 112 * la + 48 : 416 + 80 * (la >> 2) - 16 * (la & 3);
            wk = opaque(fgb + 2 * ((r == 0 ? y0 : (r == 1 ? y1 : y2)) + 4 * lg));
        }
        unsigned pw = opaque(tb + 16 * lane);
        unsigned pe = opaque(sb + P_OFF + c4 * 2048 + 16 * lane);
        COMMON_SETUP();
        q.B3 = band(la, 8 * lg, r == 2 ? 6 : 7, r == 2 ? 2 : 7, false);
        q.B4 = band(la, 8 * lg, 6, 9, false);
        asm volatile("s_mov_b32 m0, %0\n s_nop 0" :: "s"(tb) : "memory");
        STAMP(3);
        asm volatile("s_waitcnt vmcnt(0)" : "+v"(ldh), "+v"(pl), "+v"(pa), "+v"(pr), "+v"(pi), "+v"(dkh) : "v"(cpf));
        const float ldtf = __uint_as_float((unsigned)ldh << 16);
        float dskf = __uint_as_float((unsigned)dkh << 16);
        const float LOG2E = 1.4426950408889634f;
        const float dt = __uint_as_float(pkbf(0.f, __builtin_amdgcn_exp2f(ldtf * LOG2E)) & 0xffff0000u);
        const float cm = -dt * LOG2E, cth = dt * 0.15915494309189535f;
        const float arp = __uint_as_float(pkbf(0.f, __builtin_amdgcn_exp2f(__uint_as_float((unsigned)pl << 16) * LOG2E)) & 0xffff0000u);
        const float m1 = arp * cm;
        const float tt = __uint_as_float((unsigned)pa << 16) * cth;
        const float th = __builtin_amdgcn_fractf(tt);
        const float th2 = __builtin_amdgcn_fractf(tt * 16.f);
        float mg = __builtin_amdgcn_exp2f(m1);
        float re = mg * __builtin_amdgcn_cosf(th), im = mg * __builtin_amdgcn_sinf(th);
        STAMP(4);
        unsigned mx = max(pr & 0x7fffu, pi & 0x7fffu);
        mx = max(mx, (unsigned)__builtin_amdgcn_update_dpp(0, (int)mx, 0x111, 0xf, 0xf, true));
        mx = max(mx, (unsigned)__builtin_amdgcn_update_dpp(0, (int)mx, 0x112, 0xf, 0xf, true));
        mx = max(mx, (unsigned)__builtin_amdgcn_update_dpp(0, (int)mx, 0x114, 0xf, 0xf, true));
        mx = max(mx, (unsigned)__builtin_amdgcn_update_dpp(0, (int)mx, 0x118, 0xf, 0xf, true));
        const h2 e1 = pkh(re, im); csq(re, im);
        const h2 e2 = pkh(re, im); csq(re, im);
        const h2 e4 = pkh(re, im); csq(re, im);
        const h2 e8 = pkh(re, im);
        SB();
        const h2 one = {(_Float16)1.f, (_Float16)0.f};
        h2 t0, t1, t2, t3;
        t0 = cmul(e2, e1); t1 = cmul(e4, e1); TW(15, one); TW(14, e1); SB();
        const h2 y6 = cmul(e4, e2); t2 = cmul(e8, e1); TW(13, e2); TW(11, e4); TW(7, e8); SB();
        const h2 y10 = cmul(e8, e2), y12 = cmul(e8, e4); TW(12, t0); TW(10, t1); SB();
        t0 = cmul(y6, e1); const h2 y14 = cmul(e8, y6); TW(9, y6); TW(6, t2); SB();
        t1 = cmul(y10, e1); t2 = cmul(y12, e1); TW(5, y10); TW(3, y12); SB();
        t3 = cmul(y14, e1);
        float mg2 = __builtin_amdgcn_exp2f(m1 * 16.f);
        float re2 = mg2 * __builtin_amdgcn_cosf(th2), im2 = mg2 * __builtin_amdgcn_sinf(th2);
        TW(8, t0); TW(1, y14); SB();
        unsigned q0 = __builtin_amdgcn_readlane(mx, 15), q1 = __builtin_amdgcn_readlane(mx, 31);
        unsigned q2 = __builtin_amdgcn_readlane(mx, 47), q3 = __builtin_amdgcn_readlane(mx, 63);
        const unsigned ex = max(max(max(q0, q1), max(q2, q3)) >> 7, 16u);
        const float sc = __uint_as_float((268u - ex) << 23);
        const float ks = dt * __uint_as_float((ex - 13u) << 23);
        const h2 x0 = pkh(__uint_as_float((unsigned)pr << 16) * sc, __uint_as_float((unsigned)pi << 16) * -sc);
        const h2 e16 = pkh(re2, im2); csq(re2, im2);
        TW(4, t1); TW(2, t2); TW(0, t3);
        u4 yf[4];
#pragma unroll
        for (int s = 0; s < 4; s++) yf[s] = ldsr<u4>(tr + 64 * s);
        const h2 e32 = pkh(re2, im2); csq(re2, im2);
        SB();
        const h2 x1 = cmulc(x0, e16), x2 = cmulc(x0, e32);
        const h2 e64 = pkh(re2, im2); csq(re2, im2);
        TW(15, x0); SB();
        const h2 x3 = cmulc(x1, e32), x4 = cmulc(x0, e64);
        const h2 e128 = pkh(re2, im2); SB();
        const h2 x5 = cmulc(x1, e64), x6 = cmulc(x2, e64); TW(14, x1); TW(13, x2); SB();
        const h2 x7 = cmulc(x3, e64); t0 = cmulc(x0, e128); TW(12, x3); TW(11, x4); SB();
        t1 = cmulc(x1, e128); t2 = cmulc(x2, e128); TW(10, x5); TW(9, x6); SB();
        t3 = cmulc(x3, e128); h2 t4 = cmulc(x4, e128); TW(8, x7); TW(7, t0); SB();
        t0 = cmulc(x5, e128); h2 t5 = cmulc(x6, e128); TW(6, t1); TW(5, t2); SB();
        t1 = cmulc(x7, e128); csq(re2, im2); TW(4, t3); TW(3, t4); SB();
        const h2 e256 = pkh(re2, im2); csq(re2, im2); TW(2, t0); TW(1, t5); SB();
        TW(0, t1);
        u4 xf[4];
#pragma unroll
        for (int s = 0; s < 4; s++) xf[s] = ldsr<u4>(tr + 64 * s);
        const h2 e512 = pkh(re2, im2); csq(re2, im2);
        const h2 p272 = cmul(e256, e16); t0 = cmul(e256, e4);
        SB();
        t1 = cmul(e256, y6); const h2 p264 = cmul(e256, e8); SB();
        t2 = cmul(e256, y10); t3 = cmul(e256, y12); TW(7, p272); TW(1, t0); SB();
        t4 = cmul(e256, y14); t5 = cmul(p272, e2); TW(2, t1); TW(3, p264); SB();
        const h2 p520 = cmul(e512, e8), p528 = cmul(e512, e16); TW(4, t2); TW(5, t3); SB();
        const h2 p516 = cmul(e512, e4), p524 = cmul(e512, y12); TW(6, t4); TW(0, t5); SB();
        const h2 e1024 = pkh(re2, im2);
        const h2 p776 = cmul(e256, p520), p784 = cmul(e256, p528); TW(12, p520); TW(8, p528); SB();
        f4 acc0 = {0.f, 0.f, 0.f, 0.f}, acc1 = acc0, acc2 = acc0;
#pragma unroll
        for (int s = 0; s < 4; s++) acc0 = mfh(yf[s], xf[s], acc0);
        t4 = cmul(e256, e2); TW(15, p516); TW(11, p524); TW(9, e256);
        t0 = cmul(e256, p516); t1 = cmul(e256, p524); TW(10, t4);
        SB();
#pragma unroll
        for (int s = 0; s < 4; s++) yf[s] = ldsr<u4>(tr + 64 * s);
        t2 = cmul(e1024, e8); t3 = cmul(e1024, p264); TW(1, p776); TW(3, p784); SB();
        acc0 *= ks;
        if (r == 2) { ldsw<f4>(pe, acc0); arrive(c.sw, 1u); }
        t4 = cmul(e1024, p520); t5 = cmul(e1024, p776); TW(0, t0); TW(2, t1); SB();
#pragma unroll
        for (int s = 0; s < 4; s++) acc1 = mfh(xf[s], yf[s], acc1);
        t0 = cmul(e1024, e16); t1 = cmul(e1024, p272); TW(4, t2); TW(5, t3); SB();
        t2 = cmul(e1024, p528); t3 = cmul(e1024, p784); TW(6, t4); TW(7, t5); SB();
        TW(8, t0); TW(9, t1); TW(10, t2); TW(11, t3);
        SB();
#pragma unroll
        for (int s = 0; s < 4; s++) yf[s] = ldsr<u4>(tr + 64 * s);
        acc1 *= ks;
        CFENCE();
        if (r == 2) { ldsw<f4>(pe + 1024, acc1); arrive(c.sw, 4u); }
        else ldsw<f4>(pw, r == 0 ? acc1 : acc0);
#pragma unroll
        for (int s = 0; s < 4; s++) acc2 = mfh(xf[s], yf[s], acc2);
        acc2 *= ks;
        STAMP(5);
        if (r == 2) {
            waitm(c.sw, S_C2);
            STAMP(6);
            f4 kk = acc2 + ldsr<f4>(pw - 8 * T_STRIDE + 1024) + ldsr<f4>(pw - 4 * T_STRIDE + 1024);
            const unsigned q01 = pkbf(kk[0], kk[1]), q23 = pkbf(kk[2], kk[3]);
            if (la < 12) put4(wk, q01, q23);
            const unsigned k1024 = __builtin_amdgcn_readlane(q01, 3) & 0xffffu;
            ldsw<unsigned>(c.sw + 32, k1024);
            arrive(c.sw, S_G2);
            __builtin_amdgcn_s_setprio(0);
#pragma unroll
            for (int m = 0; m < 4; m++) c.BF[m] = __umul24((q.B3[m] >> 7) & 0x10001u, k1024);
            STAMP(7);
            waitm(c.sw, S_F | S_G1 | S_W01 | S_W23 | S_W45);
            STAMP(8);
            conv_pair<4>(c);
            EPILOGUE(2);
            KILLALL();
        }
        if (r == 1) {
            ldsw<f4>(pw + 1024, acc2);
            arrive(c.sw, 1u + 16u);
            const unsigned wpt = opaque(fgb + (CS + 2) * (lane & 3) + (la < 12 ? F_REL + 54 - 2 * (la >> 2) : 32));
            waitm(c.sw, S_C1);
            STAMP(6);
            f4 kk = acc1 + ldsr<f4>(pw - 4 * T_STRIDE) + ldsr<f4>(pe + 1024);
            unsigned q01 = pkbf(kk[0], kk[1]), q23 = pkbf(kk[2], kk[3]);
            const unsigned k256 = __builtin_amdgcn_readlane(q23, 57) >> 16;
            const unsigned k258 = __builtin_amdgcn_readlane(q23, 58) >> 16;
            const unsigned k512 = __builtin_amdgcn_readlane(q01, 7);
            q01 = __builtin_amdgcn_update_dpp(q01, q01, 0xC0, 0xf, 0xc, false);
            q23 = __builtin_amdgcn_update_dpp(q23, q23, 0xC0, 0xf, 0xc, false);
            put4(wk, q01, q23);
            CFENCE();
            ldsw<unsigned short>(wpt, (unsigned short)(la < 8 ? k256 : (la < 12 ? k258 : k512)));
            arrive(c.sw, S_G1);
            __builtin_amdgcn_s_setprio(1);
            STAMP(7);
            waitm(c.sw, S_U(1));
            build<2>(c, q, ww, la);
            arrive(c.sw, S_W23);
            waitm(c.sw, S_F | S_W01);
            STAMP(8);
            conv_pair<2>(c);
            EPILOGUE(1);
            KILLALL();
        }
        {
            ldsw<f4>(pw + 1024, acc2);
            arrive(c.sw, 4u + 16u);
            __builtin_amdgcn_s_setprio(1);
            waitm(c.sw, S_U(0));
            STAMP(6);
            build<0>(c, q, ww, la);
            arrive(c.sw, S_W01);
            u4 fu[17];
#pragma unroll
            for (int i = 0; i < 17; i++) fu[i] = ufrag(c.uf, 0, 266 - 32 * i);
            const u4 va = utr(c, 0, 18, 6), vb = utr(c, 0, 10, 14), vc = wtr(c, 0, 4, 12), vd = wtr(c, 0, 8, 16);
            STAMP(7);
            waitm(c.sw, S_C0);
            f4 kk = acc0 + ldsr<f4>(pw + 4 * T_STRIDE) + ldsr<f4>(pe);
            kk[3] += lane == 63 ? dskf : 0.f;
            put4(wk, pkbf(kk[0], kk[1]), pkbf(kk[2], kk[3]));
            arrive(c.sw, S_F);
            STAMP(8);
            f4 a0 = {0.f, 0.f, 0.f, 0.f}, a1 = a0;
            u4 F[4];
#pragma unroll
            for (int i = 0; i < 4; i++) F[i] = ffrag(c.fa, i);
#pragma unroll
            for (int i = 0; i < 4; i++) { a0 = mfb(F[i], fu[8 - i], a0); a1 = mfb(F[i], fu[16 - i], a1); F[i] = ffrag(c.fa, 4 + i); }
#pragma unroll
            for (int i = 0; i < 4; i++) { a0 = mfb(F[i], fu[4 - i], a0); a1 = mfb(F[i], fu[12 - i], a1); }
            waitm(c.sw, S_G1);
            F[0] = ffrag(c.fa, 8);
            const u4 k0 = gfrag(c.ga, YT(18), YT(6), 1), k1 = gfrag(c.ga, YT(10), YT(14), 1);
            const u4 k2 = gfrag(c.ga, YT(4), YT(12), 1), k3 = gfrag(c.ga, YT(8), YT(16), 1);
            a0 = mfb(F[0], fu[0], a0); a1 = mfb(F[0], fu[8], a1);
            a1 = mfb(va, k0, a1); a1 = mfb(vb, k1, a1); a1 = mfb(vc, k2, a1); a1 = mfb(vd, k3, a1);
            ldsw<u2>(c.st, cv4(a0));
            ldsw<u2>(c.st + 512, cv4(a1));
            EPILOGUE(0);
        }
    }
}

extern "C" void kernel_launch(void* const* d_in, const int* in_sizes, int n_in, void* d_out, int out_size, void* d_ws, size_t ws_size,
                              hipStream_t stream) {
    s4d_kernel<<<160, 1024, 0, stream>>>((const unsigned short*)d_in[0], (const unsigned short*)d_in[1], (const unsigned short*)d_in[2],
                                         (const unsigned short*)d_in[3], (const unsigned short*)d_in[4], (const unsigned short*)d_in[5],
                                         (const unsigned short*)d_in[6], (unsigned short*)d_out
    );
}
```

```cpp
#include <hip/hip_runtime.h>
#include <stdint.h>

typedef _Float16 h2 __attribute__((ext_vector_type(2)));
typedef _Float16 h8 __attribute__((ext_vector_type(8)));
typedef __bf16 b2 __attribute__((ext_vector_type(2)));
typedef __bf16 b8 __attribute__((ext_vector_type(8)));
typedef float f2 __attribute__((ext_vector_type(2)));
typedef float f4 __attribute__((ext_vector_type(4)));
typedef unsigned u2 __attribute__((ext_vector_type(2)));
typedef unsigned u4 __attribute__((ext_vector_type(4)));
typedef unsigned long long l8 __attribute__((ext_vector_type(8)));
typedef short s4 __attribute__((ext_vector_type(4)));
#define AS3 __attribute__((address_space(3)))
#define DEV __device__ __forceinline__
#define SB() __builtin_amdgcn_sched_barrier(0)
#define KILL(x) asm volatile("" : "=v"(x))
#define CFENCE() asm volatile("" ::: "memory")

#define NN 192
#define XPRIO 1
#define CODE_BYTES 12288

#define T_STRIDE 4608
#define U_OFF 55296
#define U_STRIDE 4640
#define FG_OFF 73856
#define CS 1984
#define FG_STRIDE (4 * CS)
#define F_REL 1360
#define MISC_OFF (FG_OFF + 4 * FG_STRIDE)
#define W_OFF (MISC_OFF + 64)
#define W_STRIDE 3648
#define P_OFF (W_OFF + 4 * W_STRIDE)
#define B_OFF (P_OFF + 4 * 6144)
#define LDS_BYTES (B_OFF + 6144)
#define S_C0 2u
#define S_C1 8u
#define S_C2 32u
#define S_F 64u
#define S_G1 128u
#define S_G2 256u
#define S_W01 512u
#define S_W23 1024u
#define S_W45 2048u
#define S_U(h) (4u << (12 + 3 * (h)))
#define S_W6 (1u << 24)

#define STAMP(i)

#define KSTAMP(i)
template <class T> DEV T ldsr(unsigned a) { return *(const AS3 T*)(uintptr_t)(a); }
template <class T> DEV T ldsrv(unsigned a) { return *(const volatile AS3 T*)(uintptr_t)(a); }
template <class T> DEV void ldsw(unsigned a, T v) { *(AS3 T*)(uintptr_t)(a) = v; }
DEV u2 ldtr(unsigned a) { s4 v = __builtin_amdgcn_ds_read_tr16_b64_v4i16((AS3 s4*)(uintptr_t)(a)); return __builtin_bit_cast(u2, v); }
DEV int opaques(int x) { asm volatile("" : "+s"(x)); return x; }
DEV unsigned opaque(unsigned x) { asm volatile("" : "+v"(x)); return x; }

DEV unsigned pkbf(float a, float b) { f2 v = {a, b}; return __builtin_bit_cast(unsigned, __builtin_convertvector(v, b2)); }
DEV h2 cmul(h2 h, h2 e) { h2 t = h * (h2){e.x, e.x}; return __builtin_elementwise_fma((h2){-h.y, h.x}, (h2){e.y, e.y}, t); }
DEV h2 cmulc(h2 h, h2 e) { h2 t = h * (h2){e.x, e.x}; return __builtin_elementwise_fma((h2){h.y, -h.x}, (h2){e.y, e.y}, t); }
DEV f4 mfb(u4 a, u4 b, f4 c) { return __builtin_amdgcn_mfma_f32_16x16x32_bf16(__builtin_bit_cast(b8, a), __builtin_bit_cast(b8, b), c, 0, 0, 0); }
DEV f4 mfh(u4 a, u4 b, f4 c) { return __builtin_amdgcn_mfma_f32_16x16x32_f16(__builtin_bit_cast(h8, a), __builtin_bit_cast(h8, b), c, 0, 0, 0); }
DEV u4 cat(u2 a, u2 b) { return (u4){a.x, a.y, b.x, b.y}; }

#define TW(row, val) asm volatile("ds_write_addtid_b32 %0 offset:%1" :: "v"(val), "i"((row) * 288) : "memory")

DEV f2 csq(f2 z) {
    f2 t, r;
    asm("v_pk_mul_f32 %0, %1, %1 op_sel_hi:[0,1]" : "=v"(t) : "v"(z));
    asm("v_pk_fma_f32 %0, %1, %1, %2 op_sel:[1,1,0] op_sel_hi:[0,1,1] neg_lo:[1,0,0]" : "=v"(r) : "v"(z), "v"(t));
    return r;
}
DEV h2 pkh(f2 v) { return __builtin_convertvector(v, h2); }

DEV void arrive(unsigned a, unsigned v) {
    asm volatile("s_mov_b64 exec, 1\n ds_add_u32 %0, %1\n s_mov_b64 exec, -1" :: "v"(a), "v"(v) : "memory");
}
DEV void arrive4(unsigned a, unsigned v) {
    asm volatile("s_mov_b64 exec, 15\n ds_add_u32 %0, %1\n s_mov_b64 exec, -1" :: "v"(a), "v"(v) : "memory");
}
DEV void waitm(unsigned a, unsigned m) {
    CFENCE();
    while ((__builtin_amdgcn_readfirstlane(ldsrv<unsigned>(a)) & m) != m) {}
    CFENCE();
}

DEV void waits(unsigned a, unsigned m) {
    CFENCE();
    while ((__builtin_amdgcn_readfirstlane(ldsrv<unsigned>(a)) & m) != m) __builtin_amdgcn_s_sleep(1);
    CFENCE();
}
DEV h2 oph(h2 x) { asm volatile("" : "+v"(x)); return x; }

DEV void put4(unsigned a, unsigned p01, unsigned p23) {
    unsigned p12 = __builtin_amdgcn_alignbit(p23, p01, 16);
    ldsw<u2>(a, (u2){p01, p23});
    ldsw<unsigned short>(a + CS + 2, (unsigned short)p01);
    ldsw<unsigned>(a + CS + 4, p12);
    ldsw<unsigned short>(a + CS + 8, (unsigned short)(p23 >> 16));
    ldsw<unsigned>(a + 2 * CS + 4, p01);
    ldsw<unsigned>(a + 2 * CS + 8, p23);
    ldsw<unsigned short>(a + 3 * CS + 6, (unsigned short)p01);
    ldsw<unsigned>(a + 3 * CS + 8, p12);
    ldsw<unsigned short>(a + 3 * CS + 12, (unsigned short)(p23 >> 16));
}

DEV void put4g(unsigned a, unsigned p01, unsigned p23) {
    unsigned p12 = __builtin_amdgcn_alignbit(p23, p01, 16);
    ldsw<u2>(a + 3 * CS + 16, (u2){p01, p23});
    ldsw<unsigned short>(a + 2, (unsigned short)p01);
    ldsw<unsigned>(a + 4, p12);
    ldsw<unsigned short>(a + 16, (unsigned short)(p23 >> 16));
    ldsw<unsigned>(a + CS + 4, p01);
    ldsw<unsigned>(a + CS + 16, p23);
    ldsw<unsigned short>(a + 2 * CS + 6, (unsigned short)p01);
    ldsw<unsigned>(a + 2 * CS + 16, p12);
    ldsw<unsigned short>(a + 2 * CS + 20, (unsigned short)(p23 >> 16));
}

struct Ctx {
    unsigned uf;
    unsigned ut;
    unsigned wt;
    unsigned fa, ga;
    unsigned sw;
    unsigned bd;
};

DEV u4 ufrag(unsigned uf, int S, int s0) { return ldsr<u4>(uf + 512 * S + 2 * (266 - s0)); }
DEV u4 gfrag(unsigned ga, int ytop, int d) { return ldsr<u4>(ga + 4 * (ytop + 1 - 16 * d)); }
DEV u4 ffrag(unsigned fa, int i) { return cat(ldsrv<u2>(fa + 544 - 64 * i), ldsrv<u2>(fa + 552 - 64 * i)); }
DEV u2 cv4(f4 d) { return (u2){pkbf(d[0], d[1]), pkbf(d[2], d[3])}; }
DEV u4 utr(const Ctx& c, int S, int p1, int p2) { return cat(ldtr(c.ut + 512 * S + 2 * (266 - p1)), ldtr(c.ut + 512 * S + 2 * (266 - p2))); }
DEV u4 wtr(const Ctx& c, int S, int p1, int p2) { return cat(ldtr(c.wt + 512 * S + 2 * (20 - p1)), ldtr(c.wt + 512 * S + 2 * (20 - p2))); }
#define KA(d) gfrag(c.ga, 47, d)
#define KB(d) gfrag(c.ga, 175, d)
#define KC(d) gfrag(c.ga, 119, d)
#define KD(d) gfrag(c.ga, 247, d)
#define KH(d) gfrag(c.ga, 383, d)
#define VA(S) utr(c, S, 18, 6)
#define VB(S) utr(c, S, 10, 14)
#define VC(S) wtr(c, S, 4, 12)
#define VD(S) wtr(c, S, 8, 16)
#define BAND(i) ldsr<u4>(c.bd + 1024 * (i))
DEV u4 bx2(u4 a, u4 b1, u4 a2, u4 b2) {
    const f4 z = {0.f, 0.f, 0.f, 0.f};
    return cat(cv4(mfb(a, b1, z)), cv4(mfb(a2, b2, z)));
}
DEV u4 and4(u4 m, unsigned v) { return (u4){m[0] & v, m[1] & v, m[2] & v, m[3] & v}; }

template <int R> DEV void near8(const Ctx& c, const u4* F, f4& a0, f4& a1) {
    u4 f0[8], f1[8];
#pragma unroll
    for (int i = 0; i < 3; i++) { f0[i] = ufrag(c.uf, R, 10 + 32 * i); f1[i] = ufrag(c.uf, R + 1, 10 + 32 * i); }
    SB();
#pragma unroll
    for (int i = 3; i < 6; i++) { f0[i] = ufrag(c.uf, R, 10 + 32 * i); f1[i] = ufrag(c.uf, R + 1, 10 + 32 * i); }
#pragma unroll
    for (int i = 0; i < 3; i++) { a0 = mfb(F[i], f0[i], a0); a1 = mfb(F[i], f1[i], a1); }
    SB();
#pragma unroll
    for (int i = 6; i < 8; i++) { f0[i] = ufrag(c.uf, R, 10 + 32 * i); f1[i] = ufrag(c.uf, R + 1, 10 + 32 * i); }
#pragma unroll
    for (int i = 3; i < 6; i++) { a0 = mfb(F[i], f0[i], a0); a1 = mfb(F[i], f1[i], a1); }
    SB();
#pragma unroll
    for (int i = 6; i < 8; i++) { a0 = mfb(F[i], f0[i], a0); a1 = mfb(F[i], f1[i], a1); }
}
template <int R> DEV void near9(const Ctx& c, const u4* F, f4& a0, f4& a1) {
    u4 f0[9], f1[8];
#pragma unroll
    for (int i = 0; i < 3; i++) { f0[i] = ufrag(c.uf, R, 10 + 32 * i); f1[i] = ufrag(c.uf, R + 1, 10 + 32 * i); }
    SB();
#pragma unroll
    for (int i = 3; i < 6; i++) { f0[i] = ufrag(c.uf, R, 10 + 32 * i); f1[i] = ufrag(c.uf, R + 1, 10 + 32 * i); }
#pragma unroll
    for (int i = 0; i < 3; i++) { a0 = mfb(F[i], f0[i], a0); a1 = mfb(F[i], f1[i], a1); }
    a1 = mfb(F[8], f0[0], a1);
    SB();
#pragma unroll
    for (int i = 6; i < 9; i++) { f0[i] = ufrag(c.uf, R, 10 + 32 * i); if (i < 8) f1[i] = ufrag(c.uf, R + 1, 10 + 32 * i); }
#pragma unroll
    for (int i = 3; i < 6; i++) { a0 = mfb(F[i], f0[i], a0); a1 = mfb(F[i], f1[i], a1); }
    SB();
#pragma unroll
    for (int i = 6; i < 9; i++) { a0 = mfb(F[i], f0[i], a0); if (i < 8) a1 = mfb(F[i], f1[i], a1); }
}
template <int S> DEV void wbuild(const Ctx& c, u4 B1, unsigned ww) {
    const f4 z = {0.f, 0.f, 0.f, 0.f};
    const u4 n0 = ufrag(c.uf, S, 10);
    u4 n1;
    if (S < 6) n1 = ufrag(c.uf, S + 1, 10);
    ldsw<u2>(ww + 512 * S, cv4(mfb(B1, n0, z)));
    if (S < 6) ldsw<u2>(ww + 512 * (S + 1), cv4(mfb(B1, n1, z)));
}

#define PROF_OUT()
#define STG(pp) (((pp) >> 1) * 8 * T_STRIDE + ((pp) & 1) * 2048)
#define STAGE(pp, x0, x1) do { ldsw<u2>(st + ((pp) & 1) * 2048, cv4(x0)); ldsw<u2>(st + ((pp) & 1) * 2048 + 576, cv4(x1)); \
    arrive(cnt, 1u << (3 * (pp))); } while (0)
#define FINISH() do { PROF_OUT(); asm volatile("s_endpgm" ::: "memory"); } while (0)

__global__ __launch_bounds__(1024, 4) void s4d_kernel(const unsigned short* __restrict__ u, const unsigned short* __restrict__ lar,
                                                      const unsigned short* __restrict__ aim, const unsigned short* __restrict__ cre,
                                                      const unsigned short* __restrict__ cim, const unsigned short* __restrict__ ldt,
                                                      const unsigned short* __restrict__ dsk, unsigned short* __restrict__ out
) {
    __shared__ __attribute__((aligned(16))) char smem[LDS_BYTES];
    const unsigned sb = (unsigned)(uintptr_t)(AS3 char*)smem;
    STAMP(0);
    const int tid = threadIdx.x;
    l8 ka;
    unsigned cpf;
    {
        unsigned long long pc;
        asm volatile("s_load_dwordx16 %0, %2, 0x0\n"
                     "s_getpc_b64 %1" : "=&s"(ka), "=&s"(pc) : "s"(__builtin_amdgcn_kernarg_segment_ptr()));
        asm volatile("v_lshlrev_b32 %0, 7, %2\n"
                     "v_min_u32 %0, %3, %0\n"
                     "global_load_dword %0, %0, %1"
                     : "=&v"(cpf) : "s"(pc), "v"(tid), "i"(CODE_BYTES));
    }
    SB();
    const int lane = tid & 63;
    const int w = __builtin_amdgcn_readfirstlane(tid >> 6);
    const int c4 = w & 3, r = w >> 2;
    const int bx = blockIdx.x;
    const int xi = bx >> 3;
    const int grp = (bx & 7) * 20 + xi;
    const int ch = grp * 4 + c4;
    const int la = lane & 15, lg = lane >> 4;

    const unsigned ub = sb + U_OFF + c4 * U_STRIDE;
    const unsigned fgb = sb + FG_OFF + c4 * FG_STRIDE;
    const f4 zf = {0.f, 0.f, 0.f, 0.f};
    Ctx c;
#define COMMON_SETUP() \
    c.uf = opaque(ub + 32 * la + 16 * lg); \
    c.ut = opaque(ub + 8 * lane); \
    c.wt = opaque(sb + W_OFF + c4 * W_STRIDE + 8 * lane); \
    c.fa = opaque(fgb + F_REL + (CS + 2) * ((la + 3) & 3) - 2 * la + 16 * lg + 2); \
    c.ga = opaque(fgb + CS * (la & 3) - 4 * (la & 12) + 16 * lg); \
    c.bd = opaque(sb + B_OFF + 16 * lane);
    unsigned pl, pa, pr, pi, ldh, dkh, uo;
    const u4 z4 = {0u, 0u, 0u, 0u};
    const unsigned za = opaque(sb + FG_OFF + tid * 16);
    if (r == 3) {
        const int lt = tid & 255;
        ldsw<u4>(za + 12288, z4);
        ldsw<u4>(za + 16384 - 960, z4);
        const unsigned zj = min((unsigned)lt >> 2, 39u);
        const unsigned z1 = opaque(U_OFF + zj * 16), z2 = opaque(U_OFF + zj * 1024 + (528 - 33 * 1024)), z3 = opaque(W_OFF + (zj - 38) * 16);
        ldsw<u4>(sb + (lt & 3) * (zj < 38 ? U_STRIDE : W_STRIDE) + (zj < 34 ? z1 : (zj < 38 ? z2 : z3)), z4);
        SB();
        const unsigned idx = min(lt * 20 + xi, 4095);
        const unsigned poff = opaque((idx >> 1) * 1280 + (bx & 7) * 160 + (idx & 1) * 156);
        uo = opaque(lt * 2560 + grp * 8);
        KILL(pa); KILL(pr); KILL(pi); KILL(ldh); KILL(dkh);
        asm volatile("s_waitcnt lgkmcnt(0)" :: "s"(ka) : "memory");
        asm volatile("global_load_dword %0, %1, %2" : "=v"(pl) : "v"(poff), "s"(ka[0]));
    } else {
        ldsw<u4>(za, z4); ldsw<u4>(za + 12288, z4);
        const unsigned vc = opaque(2 * ch), vo = opaque(2 * (ch * NN + r * 64 + lane));
        if (w < 6) {
            const unsigned pat = (unsigned)((w < 4 ? 0x3F801F00001F00E0ull : 0xC07FC0ull) >> (16 * c4)) & 0xffffu;
            const unsigned val = w == 5 ? 0xffffu : 0x3F80u;
            const unsigned by = (pat << la >> (8 * lg)) & 0xffu;
            const unsigned e = by | (by << 15);
            u4 bq;
#pragma unroll
            for (int m = 0; m < 4; m++) bq[m] = __umul24((e >> (2 * m)) & 0x10001u, val);
            ldsw<u4>(za + (B_OFF - FG_OFF), bq);
        }
        ldh = opaque(0u); pl = opaque(0u); pa = opaque(0u); pr = opaque(0u); pi = opaque(0u); dkh = opaque(0u);
        SB();
        asm volatile("s_waitcnt lgkmcnt(0)" :: "s"(ka) : "memory");
        asm volatile("global_load_short_d16_hi %0, %1, %2" : "+v"(ldh) : "v"(vc), "s"(ka[5]));
        asm volatile("global_load_short_d16_hi %0, %1, %2" : "+v"(pl) : "v"(vo), "s"(ka[1]));
        asm volatile("global_load_short_d16_hi %0, %1, %2" : "+v"(pa) : "v"(vo), "s"(ka[2]));
        asm volatile("global_load_short_d16_hi %0, %1, %2" : "+v"(pr) : "v"(vo), "s"(ka[3]));
        asm volatile("global_load_short_d16_hi %0, %1, %2" : "+v"(pi) : "v"(vo), "s"(ka[4]));
        asm volatile("global_load_short_d16_hi %0, %1, %2" : "+v"(dkh) : "v"(vc), "s"(ka[6]));
        KILL(uo);
    }
    STAMP(1);
    asm volatile("s_barrier" ::: "memory");
    STAMP(2);
    SB();
    if (opaques(r) == 3) {
        __builtin_amdgcn_s_setprio(3);
        u2 ua[2], uc[2], ud[2];
#define ULOAD(x) \
        asm volatile("global_load_dwordx2 %0, %1, %2" : "=v"(x[0]) : "v"(uo), "s"(ka[0])); \
        asm volatile("global_load_dwordx2 %0, %1, %2 offset:1280" : "=v"(x[1]) : "v"(uo), "s"(ka[0])); \
        uo += 512 * 1280;
#define UW(cc, h, val) asm volatile("ds_write_addtid_b32 %0 offset:%1" :: "v"(val), "i"((cc) * U_STRIDE + 1024 * (h)) : "memory")
#define UPUT(x, h, n) \
        asm volatile("s_waitcnt vmcnt(" #n ")" : "+v"(x[0]), "+v"(x[1])); \
        UW(0, h, __builtin_amdgcn_perm(x[1][0], x[0][0], 0x05040100u)); \
        UW(1, h, __builtin_amdgcn_perm(x[1][0], x[0][0], 0x07060302u)); \
        UW(2, h, __builtin_amdgcn_perm(x[1][1], x[0][1], 0x05040100u)); \
        UW(3, h, __builtin_amdgcn_perm(x[1][1], x[0][1], 0x07060302u)); \
        arrive4(sw4, 1u << (12 + 3 * (h))); SB();
        ULOAD(ua);
        ULOAD(uc);
        SB();
        c.uf = opaque(ub + 32 * la + 16 * lg);
        c.sw = opaque(sb + MISC_OFF + 4 * c4);
        c.bd = opaque(sb + B_OFF + 16 * lane);
        const unsigned sw4 = opaque(sb + MISC_OFF + 4 * lane);
        const unsigned ww = opaque(sb + W_OFF + 32 + c4 * W_STRIDE + 32 * la + 8 * lg);
        const u4 B1 = BAND(0);
        asm volatile("s_mov_b32 m0, %0\n s_nop 0" :: "s"(sb + U_OFF + 532 + 256 * c4) : "memory");
        SB();
        ULOAD(ud);
        STAMP(3);
        UPUT(ua, 0, 4);
        asm volatile("" :: "v"(pl), "v"(cpf));
        STAMP(4);
        ULOAD(ua); SB();
        UPUT(uc, 1, 4);
        waitm(c.sw, S_U(0)); wbuild<0>(c, B1, ww); arrive(c.sw, S_W01); SB();
        UPUT(ud, 2, 2);
        STAMP(5);
        waitm(c.sw, S_U(1)); wbuild<2>(c, B1, ww); arrive(c.sw, S_W23); SB();
        UPUT(ua, 3, 0);
        STAMP(6);
        waitm(c.sw, S_U(2)); wbuild<4>(c, B1, ww); arrive(c.sw, S_W45); SB();
        waitm(c.sw, S_U(3)); wbuild<6>(c, B1, ww); arrive(c.sw, S_W6);
        STAMP(7);
        FINISH();
        KILL(cpf); KILL(pl); KILL(pa); KILL(pr); KILL(pi); KILL(ldh); KILL(dkh);
    }
    {
        __builtin_amdgcn_s_setprio(3);
        const unsigned tb = sb + w * T_STRIDE;
        const unsigned tr = opaque(tb + 288 * la + 16 * lg);
        const unsigned pw = opaque(sb + P_OFF + c4 * 6144 + 16 * lane);
        const unsigned pw0 = opaque(pw + (r >> 1) * 1024), pw2 = opaque(pw + (r & 1) * 1024);
        unsigned wk, wpt, st;
        c.sw = opaque(sb + MISC_OFF + 4 * c4);
        if (r == 1) {
            const unsigned u0 = __builtin_amdgcn_perm(0x6D492D48u, 0x6C092C08u, la & 7), u1 = __builtin_amdgcn_perm(0x24406401u, 0x25654100u, la & 7);
            wk = opaque(fgb + 8 * ((la < 8 ? u0 : u1) & 0xffu) + 16 * lg);
            const unsigned wa = opaque((CS + 2) * (lane & 3) + F_REL + 54 - 2 * (la >> 2)), wb = opaque(CS * (lane & 3) + ((lane & 3) == 3 ? 80 : 66 + 2 * (lane & 3)));
            wpt = opaque(fgb + (la < 12 ? wa : wb));
            KILL(st); KILL(c.uf); KILL(c.ut); KILL(c.wt); KILL(c.fa); KILL(c.ga); KILL(c.bd);
        } else {
            COMMON_SETUP();
            st = opaque(tb + 16 * c4 + 8 * la + 128 * lg + 64 * (lg >> 1));
            const unsigned u0 = __builtin_amdgcn_perm(0x80889098u, 0x5D1D5C1Cu, la & 7), u1 = __builtin_amdgcn_perm(0u, 0x81899199u, la & 3);
            const unsigned w2 = opaque(8 * ((la < 8 ? u0 : u1) & 0xffu) + 16 * lg), w0 = opaque(F_REL + 56 + 32 * la + 8 * lg);
            wk = opaque(fgb + (r == 0 ? w0 : w2));
            KILL(wpt);
        }
        const unsigned cnt = opaque(sb + MISC_OFF + 16);
        asm volatile("s_mov_b32 m0, %0\n s_nop 0" :: "s"(tb) : "memory");
        STAMP(3);
        asm volatile("s_waitcnt vmcnt(0)" : "+v"(ldh), "+v"(pl), "+v"(pa), "+v"(pr), "+v"(pi), "+v"(dkh) : "v"(cpf));
        const float LOG2E = 1.4426950408889634f;
        const float dt = __uint_as_float(pkbf(0.f, __builtin_amdgcn_exp2f(__uint_as_float(ldh) * LOG2E)));
        const float arp = __uint_as_float(pkbf(0.f, __builtin_amdgcn_exp2f(__uint_as_float(pl) * LOG2E)));
        const float m1 = arp * (-dt * LOG2E);
        const float th = __builtin_amdgcn_fractf(__uint_as_float(pa) * (dt * 0.15915494309189535f));
        const float mg = __builtin_amdgcn_exp2f(m1);
        f2 z = (f2){__builtin_amdgcn_cosf(th), __builtin_amdgcn_sinf(th)} * mg;
        STAMP(4);
        unsigned mx;
        asm("v_max_f32 %0, |%1|, |%2|\n s_nop 1" : "=v"(mx) : "v"(pr), "v"(pi));
#define MXS(ctl, rm) mx = max(mx, (unsigned)__builtin_amdgcn_update_dpp(0, (int)mx, ctl, rm, 0xf, true))
        const h2 e1 = pkh(z); MXS(0x111, 0xf); z = csq(z); SB();
        const h2 e2 = pkh(z); MXS(0x112, 0xf); z = csq(z); SB();
        const h2 e4 = pkh(z); MXS(0x114, 0xf); z = csq(z); SB();
        const h2 e8 = pkh(z);
        const h2 one = {(_Float16)1.f, (_Float16)0.f};
        h2 t0, t1, t2, t3;
        t0 = cmul(e2, e1); MXS(0x118, 0xf); t1 = cmul(e4, e1); TW(15, one); TW(14, e1); SB();
        const h2 y6 = cmul(e4, e2); MXS(0x142, 0xa); t2 = cmul(e8, e1); TW(13, e2); TW(11, e4); TW(7, e8); SB();
        const h2 y10 = cmul(e8, e2); MXS(0x143, 0xc); const h2 y12 = cmul(e8, e4); TW(12, t0); TW(10, t1); SB();
        const float th2 = th * 16.f;
        const float mg2 = __builtin_amdgcn_exp2f(m1 * 16.f);
        t0 = cmul(y6, e1); const h2 y14 = cmul(e8, y6); TW(9, y6); TW(6, t2); SB();
        z = (f2){__builtin_amdgcn_cosf(th2), __builtin_amdgcn_sinf(th2)} * mg2;
        const unsigned ex = max((unsigned)__builtin_amdgcn_readlane(mx, 63) >> 23, 16u);
        const float sc = __uint_as_float((268u - ex) << 23);
        const float ks = dt * __uint_as_float((ex - 13u) << 23);
        t1 = cmul(y10, e1); t2 = cmul(y12, e1); TW(5, y10); TW(3, y12); SB();
        t3 = cmul(y14, e1);
        TW(8, t0); TW(1, y14); SB();
        const h2 x0 = pkh((f2){__uint_as_float(pr), -__uint_as_float(pi)} * sc);
        const h2 e16 = pkh(z); z = csq(z);
        TW(4, t1); TW(2, t2); TW(0, t3);
        u4 yf[4];
#pragma unroll
        for (int s = 0; s < 4; s++) yf[s] = ldsr<u4>(tr + 64 * s);
        const h2 e32 = pkh(z); z = csq(z);
        SB();
        const h2 x1 = cmulc(x0, e16), x2 = cmulc(x0, e32);
        const h2 e64 = pkh(z); z = csq(z);
        TW(15, x0); SB();
        const h2 x3 = cmulc(x1, e32), x4 = cmulc(x0, e64);
        const h2 e128 = pkh(z); SB();
        const h2 x5 = cmulc(x1, e64), x6 = cmulc(x2, e64); TW(14, x1); TW(13, x2); SB();
        const h2 x7 = cmulc(x3, e64); t0 = cmulc(x0, e128); TW(12, x3); TW(11, x4); SB();
        t1 = cmulc(x1, e128); t2 = cmulc(x2, e128); TW(10, x5); TW(9, x6); SB();
        t3 = cmulc(x3, e128); h2 t4 = cmulc(x4, e128); TW(8, x7); TW(7, t0); SB();
        t0 = cmulc(x5, e128); h2 t5 = cmulc(x6, e128); TW(6, t1); TW(5, t2); SB();
        t1 = cmulc(x7, e128); TW(4, t3); TW(3, t4); SB();
        z = csq(z); TW(2, t0); TW(1, t5); SB();
        TW(0, t1);
        u4 xf[4];
#pragma unroll
        for (int s = 0; s < 4; s++) xf[s] = ldsr<u4>(tr + 64 * s);
        const h2 e256 = pkh(z); z = csq(z);
        const h2 p272 = cmul(e256, e16); t0 = cmul(e256, e4);
        SB();
        t1 = cmul(e256, y6); const h2 p264 = cmul(e256, e8); SB();
        f4 acc0 = zf, acc1 = zf, acc2 = zf;
#pragma unroll
        for (int s = 0; s < 4; s++) acc0 = mfh(yf[s], xf[s], acc0);
        t2 = cmul(e256, y10); t3 = cmul(e256, y12); TW(7, p272); TW(1, t0); SB();
        t4 = cmul(e256, y14); t5 = cmul(p272, e2); TW(2, t1); TW(3, p264); SB();
        const h2 e512 = pkh(z); z = csq(z);
        acc0 *= ks;
        STAMP(5);
        asm volatile("s_cmp_eq_u32 %0, 0\n s_cbranch_scc1 .Lp0_%=\n ds_write_b128 %1, %2\n s_mov_b64 exec, 1\n ds_add_u32 %3, %4\n s_mov_b64 exec, -1\n"
                     ".Lp0_%=:\n s_cmp_eq_u32 %0, 2\n s_cbranch_scc1 .Lp1_%=\n s_setprio 2\n .Lp1_%=:"
                     :: "s"(r), "v"(pw0), "v"(acc0), "v"(c.sw), "v"(1u) : "scc", "memory");
        SB();
        const h2 p520 = cmul(e512, e8), p528 = cmul(e512, e16); TW(4, t2); TW(5, t3); SB();
        const h2 p516 = cmul(e512, e4), p524 = cmul(e512, y12); TW(6, t4); TW(0, t5); SB();
        const h2 e1024 = pkh(z);
        t4 = cmul(e256, e2); TW(12, p520); TW(8, p528); TW(9, e256); SB();
        const h2 p776 = cmul(e256, p520), p784 = cmul(e256, p528); TW(15, p516); TW(11, p524); TW(10, t4);
        SB();
#pragma unroll
        for (int s = 0; s < 4; s++) yf[s] = ldsr<u4>(tr + 64 * s);
        t0 = cmul(e256, p516); t1 = cmul(e256, p524);
        t2 = cmul(e1024, e8); t3 = cmul(e1024, p264); TW(1, p776); TW(3, p784); SB();
#pragma unroll
        for (int s = 0; s < 4; s++) acc1 = mfh(xf[s], yf[s], acc1);
        t4 = cmul(e1024, p520); t5 = cmul(e1024, p776); TW(0, t0); TW(2, t1); SB();
        t0 = cmul(e1024, e16); t1 = cmul(e1024, p272); TW(4, t2); TW(5, t3); SB();
        t2 = cmul(e1024, p528); t3 = cmul(e1024, p784); TW(6, t4); TW(7, t5); SB();
        TW(8, t0); TW(9, t1); TW(10, t2); TW(11, t3);
        SB();
#pragma unroll
        for (int s = 0; s < 4; s++) yf[s] = ldsr<u4>(tr + 64 * s);
        acc1 *= ks;
        SB();
        if (r != 1) { ldsw<f4>(pw0 + 2048, acc1); arrive(c.sw, 4u); }
        KSTAMP(8);
        SB();
#pragma unroll
        for (int s = 0; s < 4; s++) acc2 = mfh(xf[s], yf[s], acc2);
        acc2 *= ks;
        if (r == 2) {
            waitm(c.sw, S_C2);
            f4 kk = acc2 + ldsr<f4>(pw + 4096) + ldsr<f4>(pw + 5120);
            const unsigned q01 = pkbf(kk[0], kk[1]), q23 = pkbf(kk[2], kk[3]);
            if (la < 12) put4g(wk, q01, q23);
            const unsigned k1024 = (__builtin_amdgcn_readlane(q01, 3) & 0xffffu) * 0x10001u;
            arrive(c.sw, S_G2);
            __builtin_amdgcn_s_setprio(1);
            STAMP(6);
            f4 a[4] = {zf, zf, zf, zf};
            u4 p[5], v[5], k[4];
            {
                const u4 B3 = BAND(3), B4 = BAND(4), BF = and4(BAND(5), k1024);
#pragma unroll
                for (int d = 0; d < 4; d++) k[d] = KH(4 + d);
                waits(c.sw, S_U(0) | S_U(1));
                u4 q[4], n[4];
#pragma unroll
                for (int S = 0; S < 4; S++) { p[S] = ufrag(c.uf, S, 18); q[S] = ufrag(c.uf, S, 26); }
#pragma unroll
                for (int S = 0; S < 4; S++) n[S] = ufrag(c.uf, S, 10);
                SB();
#pragma unroll
                for (int S = 0; S < 4; S++) v[S] = bx2(p[S], B3, q[S], B4);
#pragma unroll
                for (int t = 0; t < 4; t++) a[t] = mfb(BF, n[t], a[t]);
            }
            const u4 B2a = BAND(1), B2b = BAND(2);
            SB();
#pragma unroll
            for (int S = 0; S < 4; S++)
#pragma unroll
                for (int t = S; t < 4; t++) a[t] = mfb(v[S], k[t - S], a[t]);
#pragma unroll
            for (int S = 0; S < 4; S++) v[S] = bx2(p[S], B2b, p[S], B2a);
            waits(c.sw, S_G1 | S_W45);
            STAMP(7);
            p[4] = ufrag(c.uf, 4, 18);
            const u4 kd3 = KD(3), kd4 = KD(4);
            u4 vc[6];
#pragma unroll
            for (int S = 0; S < 6; S++) vc[S] = VC(S);
#pragma unroll
            for (int d = 0; d < 4; d++) k[d] = KC(4 - d);
            SB();
            v[4] = bx2(p[4], B2b, p[4], B2a);
#pragma unroll
            for (int t = 0; t < 4; t++) a[t] = mfb(v[t], kd4, a[t]);
#pragma unroll
            for (int t = 0; t < 4; t++) a[t] = mfb(v[t + 1], kd3, a[t]);
            SB();
            u4 k1[4], k2;
#pragma unroll
            for (int S = 0; S < 4; S++) v[S] = VA(2 + S);
            k2 = KA(2); k1[0] = KA(1);
#pragma unroll
            for (int d = 0; d < 4; d++)
#pragma unroll
                for (int t = 0; t < 4; t++) if (t + d < 6) a[t] = mfb(vc[t + d], k[d], a[t]);
            k1[2] = k[3];
            SB();
#pragma unroll
            for (int S = 0; S < 4; S++) p[S] = VB(2 + S);
            k[0] = KB(2); k1[1] = KB(1);
#pragma unroll
            for (int t = 0; t < 4; t++) a[t] = mfb(v[t], k2, a[t]);
#pragma unroll
            for (int t = 0; t < 3; t++) a[t] = mfb(v[t + 1], k1[0], a[t]);
            SB();
#pragma unroll
            for (int S = 0; S < 4; S++) v[S] = VD(2 + S);
            k2 = KD(2); k1[3] = KD(1);
#pragma unroll
            for (int t = 0; t < 4; t++) a[t] = mfb(p[t], k[0], a[t]);
#pragma unroll
            for (int t = 0; t < 3; t++) a[t] = mfb(p[t + 1], k1[1], a[t]);
            SB();
            waitm(c.sw, S_F);
            u4 F[9];
#pragma unroll
            for (int i = 0; i < 9; i++) F[i] = ffrag(c.fa, i);
#pragma unroll
            for (int t = 0; t < 4; t++) a[t] = mfb(v[t], k2, a[t]);
#pragma unroll
            for (int t = 0; t < 3; t++) a[t] = mfb(v[t + 1], k1[3], a[t]);
            STAMP(8);
            near9<4>(c, F, a[0], a[1]);
            STAGE(2, a[0], a[1]);
            STAMP(9);
            waitm(c.sw, S_W6);
            near9<6>(c, F, a[2], a[3]);
            {
                const u4 va = VA(6), vb = VB(6), vc6 = VC(6), vd = VD(6);
                a[3] = mfb(va, k1[0], a[3]); a[3] = mfb(vb, k1[1], a[3]); a[3] = mfb(vc6, k1[2], a[3]); a[3] = mfb(vd, k1[3], a[3]);
            }
            STAGE(3, a[2], a[3]);
            STAMP(10);
            FINISH();
            KILL(acc0); KILL(acc1); KILL(acc2); KILL(wk); KILL(wpt); KILL(dkh);
        }
        ldsw<f4>(pw2 + 4096, acc2);
        arrive(c.sw, 16u);
        __builtin_amdgcn_s_setprio(3);
        KSTAMP(7);
        if (r == 1) {
            waitm(c.sw, S_C1);
            f4 kk = acc1 + ldsr<f4>(pw + 2048) + ldsr<f4>(pw + 3072);
            unsigned q01 = pkbf(kk[0], kk[1]), q23 = pkbf(kk[2], kk[3]);
            const unsigned k256 = __builtin_amdgcn_readlane(q23, 57) >> 16;
            const unsigned k258 = __builtin_amdgcn_readlane(q23, 58) >> 16;
            const unsigned k512 = __builtin_amdgcn_readlane(q01, 7);
            q01 = __builtin_amdgcn_update_dpp(q01, q01, 0xC0, 0xf, 0xc, false);
            q23 = __builtin_amdgcn_update_dpp(q23, q23, 0xC0, 0xf, 0xc, false);
            put4g(wk, q01, q23);
            CFENCE();
            ldsw<unsigned short>(wpt, (unsigned short)(la < 8 ? k256 : (la < 12 ? k258 : k512)));
            arrive(c.sw, S_G1);
            STAMP(6);
            const unsigned ea = opaque(sb + ((lane >> 2) & 3) * (T_STRIDE + 16) + 8 * lg + 128 * (lane & 3) + 32 * (lane & 2) + 64 * (c4 & 1) + 576 * (c4 >> 1));
            auto ro = __builtin_amdgcn_make_buffer_rsrc((void*)ka[7], (short)0, 2048 * 1280, 0x00020000);
            const unsigned vo = opaque((lane + 128 * c4) * 1280 + grp * 8);
#pragma unroll
            for (int pp = 0; pp < 4; pp++) {
                waits(cnt, 4u << (3 * pp));
                const u2 v0 = ldtr(ea + STG(pp)), v1 = ldtr(ea + STG(pp) + 32);
                __builtin_amdgcn_raw_buffer_store_b64(v0, ro, vo, 512 * pp * 1280, 0);
                __builtin_amdgcn_raw_buffer_store_b64(v1, ro, vo, (512 * pp + 64) * 1280, 0);
                STAMP(7 + pp);
            }
            FINISH();
            KILL(acc0); KILL(wk); KILL(dkh);
        }
        {
            waitm(c.sw, S_C0);
            f4 kk = acc0 + ldsr<f4>(pw) + ldsr<f4>(pw + 1024);
            kk[3] += lane == 63 ? __uint_as_float(dkh) : 0.f;
            put4(wk, pkbf(kk[0], kk[1]), pkbf(kk[2], kk[3]));
            arrive(c.sw, S_F);
            __builtin_amdgcn_s_setprio(XPRIO);
            STAMP(6);
            f4 a[4] = {zf, zf, zf, zf};
            u4 F[8];
#pragma unroll
            for (int i = 0; i < 8; i++) F[i] = ffrag(c.fa, i);
            waits(c.sw, S_U(0));
            STAMP(7);
            near8<0>(c, F, a[0], a[1]);
            waits(c.sw, S_U(1));
            near8<2>(c, F, a[2], a[3]);
            u4 n[4];
#pragma unroll
            for (int t = 0; t < 4; t++) n[t] = ufrag(c.uf, t, 266);
            const u4 va0 = VA(0), vb0 = VB(0);
            waits(c.sw, S_G1 | S_W01);
            STAMP(8);
            __builtin_amdgcn_s_setprio(2);
            const u4 F8 = ffrag(c.fa, 8), ka1 = KA(1), kb1 = KB(1), vc0 = VC(0), kc1 = KC(1), vd0 = VD(0), kd1 = KD(1);
            SB();
            const u4 va1 = VA(1), vb1 = VB(1), vc1 = VC(1), vd1 = VD(1), ka2 = KA(2), kb2 = KB(2), kc2 = KC(2), kd2 = KD(2);
            a[1] = mfb(va0, ka1, a[1]); a[0] = mfb(F8, n[0], a[0]); a[1] = mfb(vb0, kb1, a[1]); a[1] = mfb(F8, n[1], a[1]);
            a[1] = mfb(vc0, kc1, a[1]); a[1] = mfb(vd0, kd1, a[1]);
            a[2] = mfb(F8, n[2], a[2]); a[3] = mfb(F8, n[3], a[3]);
            STAGE(0, a[0], a[1]);
            STAMP(9);
            const u4 B2a = BAND(1), B2b = BAND(2), p0 = ufrag(c.uf, 0, 18), va2 = VA(2), vb2 = VB(2);
            a[2] = mfb(va1, ka1, a[2]); a[3] = mfb(va1, ka2, a[3]); a[2] = mfb(va0, ka2, a[2]);
            a[2] = mfb(vb1, kb1, a[2]); a[3] = mfb(vb1, kb2, a[3]); a[2] = mfb(vb0, kb2, a[2]);
            waitm(c.sw, S_G2 | S_W23);
            const u4 vc2 = VC(2), vd2 = VD(2), kc3 = KC(3), kd3 = KD(3);
            const u4 w0 = bx2(p0, B2b, p0, B2a);
            a[2] = mfb(vc1, kc1, a[2]); a[3] = mfb(vc1, kc2, a[3]); a[2] = mfb(vc0, kc2, a[2]);
            a[2] = mfb(vd1, kd1, a[2]); a[3] = mfb(vd1, kd2, a[3]); a[2] = mfb(vd0, kd2, a[2]);
            a[3] = mfb(va2, ka1, a[3]); a[3] = mfb(vb2, kb1, a[3]); a[3] = mfb(vc2, kc1, a[3]); a[3] = mfb(vd2, kd1, a[3]);
            a[3] = mfb(vc0, kc3, a[3]); a[3] = mfb(w0, kd3, a[3]);
            STAGE(1, a[2], a[3]);
            STAMP(10);
            FINISH();
        }
    }
}

extern "C" void kernel_launch(void* const* d_in, const int* in_sizes, int n_in, void* d_out, int out_size, void* d_ws, size_t ws_size,
                              hipStream_t stream) {
    s4d_kernel<<<160, 1024, 0, stream>>>((const unsigned short*)d_in[0], (const unsigned short*)d_in[1], (const unsigned short*)d_in[2],
                                         (const unsigned short*)d_in[3], (const unsigned short*)d_in[4], (const unsigned short*)d_in[5],
                                         (const unsigned short*)d_in[6], (unsigned short*)d_out
    );
}
```

```cpp
#define TSPIN 0
#define TROLE -1
#include <hip/hip_runtime.h>
#include <stdint.h>

typedef _Float16 h2 __attribute__((ext_vector_type(2)));
typedef _Float16 h8 __attribute__((ext_vector_type(8)));
typedef __bf16 b2 __attribute__((ext_vector_type(2)));
typedef __bf16 b8 __attribute__((ext_vector_type(8)));
typedef float f2 __attribute__((ext_vector_type(2)));
typedef float f4 __attribute__((ext_vector_type(4)));
typedef unsigned u2 __attribute__((ext_vector_type(2)));
typedef unsigned u4 __attribute__((ext_vector_type(4)));
typedef unsigned long long l8 __attribute__((ext_vector_type(8)));
typedef short s4 __attribute__((ext_vector_type(4)));
#define AS3 __attribute__((address_space(3)))
#define DEV __device__ __forceinline__
#define SB() __builtin_amdgcn_sched_barrier(0)
#define KILL(x) asm volatile("" : "=v"(x))
#define CFENCE() asm volatile("" ::: "memory")

#define NN 192
#define XPRIO 1
#define CODE_BYTES 12288

#define T_STRIDE 4608
#define U_OFF 55296
#define U_STRIDE 4640
#define FG_OFF 73856
#define CS 1984
#define FG_STRIDE (4 * CS)
#define F_REL 1360
#define MISC_OFF (FG_OFF + 4 * FG_STRIDE)
#define W_OFF (MISC_OFF + 64)
#define W_STRIDE 3648
#define P_OFF (W_OFF + 4 * W_STRIDE)
#define B_OFF (P_OFF + 4 * 6144)
#define LDS_BYTES (B_OFF + 6144)
#define S_C0 2u
#define S_C1 8u
#define S_C2 32u
#define S_F 64u
#define S_G1 128u
#define S_G2 256u
#define S_W01 512u
#define S_W23 1024u
#define S_W45 2048u
#define S_U(h) (4u << (12 + 3 * (h)))
#define S_W6 (1u << 24)

#define STAMP(i) do { if ((i) == 0 || (i) == TSPIN) { SB(); ts[i] = __builtin_amdgcn_s_memrealtime(); SB(); } } while (0)

#define KSTAMP(i)
template <class T> DEV T ldsr(unsigned a) { return *(const AS3 T*)(uintptr_t)(a); }
template <class T> DEV T ldsrv(unsigned a) { return *(const volatile AS3 T*)(uintptr_t)(a); }
template <class T> DEV void ldsw(unsigned a, T v) { *(AS3 T*)(uintptr_t)(a) = v; }
DEV u2 ldtr(unsigned a) { s4 v = __builtin_amdgcn_ds_read_tr16_b64_v4i16((AS3 s4*)(uintptr_t)(a)); return __builtin_bit_cast(u2, v); }
DEV int opaques(int x) { asm volatile("" : "+s"(x)); return x; }
DEV unsigned opaque(unsigned x) { asm volatile("" : "+v"(x)); return x; }

DEV unsigned pkbf(float a, float b) { f2 v = {a, b}; return __builtin_bit_cast(unsigned, __builtin_convertvector(v, b2)); }
DEV h2 cmul(h2 h, h2 e) { h2 t = h * (h2){e.x, e.x}; return __builtin_elementwise_fma((h2){-h.y, h.x}, (h2){e.y, e.y}, t); }
DEV h2 cmulc(h2 h, h2 e) { h2 t = h * (h2){e.x, e.x}; return __builtin_elementwise_fma((h2){h.y, -h.x}, (h2){e.y, e.y}, t); }
DEV f4 mfb(u4 a, u4 b, f4 c) { return __builtin_amdgcn_mfma_f32_16x16x32_bf16(__builtin_bit_cast(b8, a), __builtin_bit_cast(b8, b), c, 0, 0, 0); }
DEV f4 mfh(u4 a, u4 b, f4 c) { return __builtin_amdgcn_mfma_f32_16x16x32_f16(__builtin_bit_cast(h8, a), __builtin_bit_cast(h8, b), c, 0, 0, 0); }
DEV u4 cat(u2 a, u2 b) { return (u4){a.x, a.y, b.x, b.y}; }

#define TW(row, val) asm volatile("ds_write_addtid_b32 %0 offset:%1" :: "v"(val), "i"((row) * 288) : "memory")

DEV f2 csq(f2 z) {
    f2 t, r;
    asm("v_pk_mul_f32 %0, %1, %1 op_sel_hi:[0,1]" : "=v"(t) : "v"(z));
    asm("v_pk_fma_f32 %0, %1, %1, %2 op_sel:[1,1,0] op_sel_hi:[0,1,1] neg_lo:[1,0,0]" : "=v"(r) : "v"(z), "v"(t));
    return r;
}
DEV h2 pkh(f2 v) { return __builtin_convertvector(v, h2); }

DEV void arrive(unsigned a, unsigned v) {
    asm volatile("s_mov_b64 exec, 1\n ds_add_u32 %0, %1\n s_mov_b64 exec, -1" :: "v"(a), "v"(v) : "memory");
}
DEV void arrive4(unsigned a, unsigned v) {
    asm volatile("s_mov_b64 exec, 15\n ds_add_u32 %0, %1\n s_mov_b64 exec, -1" :: "v"(a), "v"(v) : "memory");
}
DEV void waitm(unsigned a, unsigned m) {
    CFENCE();
    while ((__builtin_amdgcn_readfirstlane(ldsrv<unsigned>(a)) & m) != m) {}
    CFENCE();
}

DEV void waits(unsigned a, unsigned m) {
    CFENCE();
    while ((__builtin_amdgcn_readfirstlane(ldsrv<unsigned>(a)) & m) != m) __builtin_amdgcn_s_sleep(1);
    CFENCE();
}
DEV h2 oph(h2 x) { asm volatile("" : "+v"(x)); return x; }

DEV void put4(unsigned a, unsigned p01, unsigned p23) {
    unsigned p12 = __builtin_amdgcn_alignbit(p23, p01, 16);
    ldsw<u2>(a, (u2){p01, p23});
    ldsw<unsigned short>(a + CS + 2, (unsigned short)p01);
    ldsw<unsigned>(a + CS + 4, p12);
    ldsw<unsigned short>(a + CS + 8, (unsigned short)(p23 >> 16));
    ldsw<unsigned>(a + 2 * CS + 4, p01);
    ldsw<unsigned>(a + 2 * CS + 8, p23);
    ldsw<unsigned short>(a + 3 * CS + 6, (unsigned short)p01);
    ldsw<unsigned>(a + 3 * CS + 8, p12);
    ldsw<unsigned short>(a + 3 * CS + 12, (unsigned short)(p23 >> 16));
}

DEV void put4g(unsigned a, unsigned p01, unsigned p23) {
    unsigned p12 = __builtin_amdgcn_alignbit(p23, p01, 16);
    ldsw<u2>(a + 3 * CS + 16, (u2){p01, p23});
    ldsw<unsigned short>(a + 2, (unsigned short)p01);
    ldsw<unsigned>(a + 4, p12);
    ldsw<unsigned short>(a + 16, (unsigned short)(p23 >> 16));
    ldsw<unsigned>(a + CS + 4, p01);
    ldsw<unsigned>(a + CS + 16, p23);
    ldsw<unsigned short>(a + 2 * CS + 6, (unsigned short)p01);
    ldsw<unsigned>(a + 2 * CS + 16, p12);
    ldsw<unsigned short>(a + 2 * CS + 20, (unsigned short)(p23 >> 16));
}

struct Ctx {
    unsigned uf;
    unsigned ut;
    unsigned wt;
    unsigned fa, ga;
    unsigned sw;
    unsigned bd;
};

DEV u4 ufrag(unsigned uf, int S, int s0) { return ldsr<u4>(uf + 512 * S + 2 * (266 - s0)); }
DEV u4 gfrag(unsigned ga, int ytop, int d) { return ldsr<u4>(ga + 4 * (ytop + 1 - 16 * d)); }
DEV u4 ffrag(unsigned fa, int i) { return cat(ldsrv<u2>(fa + 544 - 64 * i), ldsrv<u2>(fa + 552 - 64 * i)); }
DEV u2 cv4(f4 d) { return (u2){pkbf(d[0], d[1]), pkbf(d[2], d[3])}; }
DEV u4 utr(const Ctx& c, int S, int p1, int p2) { return cat(ldtr(c.ut + 512 * S + 2 * (266 - p1)), ldtr(c.ut + 512 * S + 2 * (266 - p2))); }
DEV u4 wtr(const Ctx& c, int S, int p1, int p2) { return cat(ldtr(c.wt + 512 * S + 2 * (20 - p1)), ldtr(c.wt + 512 * S + 2 * (20 - p2))); }
#define KA(d) gfrag(c.ga, 47, d)
#define KB(d) gfrag(c.ga, 175, d)
#define KC(d) gfrag(c.ga, 119, d)
#define KD(d) gfrag(c.ga, 247, d)
#define KH(d) gfrag(c.ga, 383, d)
#define VA(S) utr(c, S, 18, 6)
#define VB(S) utr(c, S, 10, 14)
#define VC(S) wtr(c, S, 4, 12)
#define VD(S) wtr(c, S, 8, 16)
#define BAND(i) ldsr<u4>(c.bd + 1024 * (i))
DEV u4 bx2(u4 a, u4 b1, u4 a2, u4 b2) {
    const f4 z = {0.f, 0.f, 0.f, 0.f};
    return cat(cv4(mfb(a, b1, z)), cv4(mfb(a2, b2, z)));
}
DEV u4 and4(u4 m, unsigned v) { return (u4){m[0] & v, m[1] & v, m[2] & v, m[3] & v}; }

template <int R> DEV void near8(const Ctx& c, const u4* F, f4& a0, f4& a1) {
    u4 f0[8], f1[8];
#pragma unroll
    for (int i = 0; i < 3; i++) { f0[i] = ufrag(c.uf, R, 10 + 32 * i); f1[i] = ufrag(c.uf, R + 1, 10 + 32 * i); }
    SB();
#pragma unroll
    for (int i = 3; i < 6; i++) { f0[i] = ufrag(c.uf, R, 10 + 32 * i); f1[i] = ufrag(c.uf, R + 1, 10 + 32 * i); }
#pragma unroll
    for (int i = 0; i < 3; i++) { a0 = mfb(F[i], f0[i], a0); a1 = mfb(F[i], f1[i], a1); }
    SB();
#pragma unroll
    for (int i = 6; i < 8; i++) { f0[i] = ufrag(c.uf, R, 10 + 32 * i); f1[i] = ufrag(c.uf, R + 1, 10 + 32 * i); }
#pragma unroll
    for (int i = 3; i < 6; i++) { a0 = mfb(F[i], f0[i], a0); a1 = mfb(F[i], f1[i], a1); }
    SB();
#pragma unroll
    for (int i = 6; i < 8; i++) { a0 = mfb(F[i], f0[i], a0); a1 = mfb(F[i], f1[i], a1); }
}
template <int R> DEV void near9(const Ctx& c, const u4* F, f4& a0, f4& a1) {
    u4 f0[9], f1[8];
#pragma unroll
    for (int i = 0; i < 3; i++) { f0[i] = ufrag(c.uf, R, 10 + 32 * i); f1[i] = ufrag(c.uf, R + 1, 10 + 32 * i); }
    SB();
#pragma unroll
    for (int i = 3; i < 6; i++) { f0[i] = ufrag(c.uf, R, 10 + 32 * i); f1[i] = ufrag(c.uf, R + 1, 10 + 32 * i); }
#pragma unroll
    for (int i = 0; i < 3; i++) { a0 = mfb(F[i], f0[i], a0); a1 = mfb(F[i], f1[i], a1); }
    a1 = mfb(F[8], f0[0], a1);
    SB();
#pragma unroll
    for (int i = 6; i < 9; i++) { f0[i] = ufrag(c.uf, R, 10 + 32 * i); if (i < 8) f1[i] = ufrag(c.uf, R + 1, 10 + 32 * i); }
#pragma unroll
    for (int i = 3; i < 6; i++) { a0 = mfb(F[i], f0[i], a0); a1 = mfb(F[i], f1[i], a1); }
    SB();
#pragma unroll
    for (int i = 6; i < 9; i++) { a0 = mfb(F[i], f0[i], a0); if (i < 8) a1 = mfb(F[i], f1[i], a1); }
}
template <int S> DEV void wbuild(const Ctx& c, u4 B1, unsigned ww) {
    const f4 z = {0.f, 0.f, 0.f, 0.f};
    const u4 n0 = ufrag(c.uf, S, 10);
    u4 n1;
    if (S < 6) n1 = ufrag(c.uf, S + 1, 10);
    ldsw<u2>(ww + 512 * S, cv4(mfb(B1, n0, z)));
    if (S < 6) ldsw<u2>(ww + 512 * (S + 1), cv4(mfb(B1, n1, z)));
}

#define PROF_OUT() \
    asm volatile("s_waitcnt vmcnt(0)" ::: "memory"); \
    STAMP(12); \
    { unsigned long long tgt = ts[0] + 3000 + (TROLE < 0 || r == TROLE ? 4 * (ts[TSPIN] - ts[0]) : 0); \
      while (__builtin_amdgcn_s_memrealtime() < tgt) __builtin_amdgcn_s_sleep(2); }
#define STG(pp) (((pp) >> 1) * 8 * T_STRIDE + ((pp) & 1) * 2048)
#define STAGE(pp, x0, x1) do { ldsw<u2>(st + ((pp) & 1) * 2048, cv4(x0)); ldsw<u2>(st + ((pp) & 1) * 2048 + 576, cv4(x1)); \
    arrive(cnt, 1u << (3 * (pp))); } while (0)
#define FINISH() do { PROF_OUT(); asm volatile("s_endpgm" ::: "memory"); } while (0)

__global__ __launch_bounds__(1024, 4) void s4d_kernel(const unsigned short* __restrict__ u, const unsigned short* __restrict__ lar,
                                                      const unsigned short* __restrict__ aim, const unsigned short* __restrict__ cre,
                                                      const unsigned short* __restrict__ cim, const unsigned short* __restrict__ ldt,
                                                      const unsigned short* __restrict__ dsk, unsigned short* __restrict__ out
) {
    __shared__ __attribute__((aligned(16))) char smem[LDS_BYTES];
    const unsigned sb = (unsigned)(uintptr_t)(AS3 char*)smem;
    unsigned long long ts[16];
    for (int i = 0; i < 16; i++) ts[i] = 0;
    STAMP(0);
    const int tid = threadIdx.x;
    l8 ka;
    unsigned cpf;
    {
        unsigned long long pc;
        asm volatile("s_load_dwordx16 %0, %2, 0x0\n"
                     "s_getpc_b64 %1" : "=&s"(ka), "=&s"(pc) : "s"(__builtin_amdgcn_kernarg_segment_ptr()));
        asm volatile("v_lshlrev_b32 %0, 7, %2\n"
                     "v_min_u32 %0, %3, %0\n"
                     "global_load_dword %0, %0, %1"
                     : "=&v"(cpf) : "s"(pc), "v"(tid), "i"(CODE_BYTES));
    }
    SB();
    const int lane = tid & 63;
    const int w = __builtin_amdgcn_readfirstlane(tid >> 6);
    const int c4 = w & 3, r = w >> 2;
    const int bx = blockIdx.x;
    const int xi = bx >> 3;
    const int grp = (bx & 7) * 20 + xi;
    const int ch = grp * 4 + c4;
    const int la = lane & 15, lg = lane >> 4;

    const unsigned ub = sb + U_OFF + c4 * U_STRIDE;
    const unsigned fgb = sb + FG_OFF + c4 * FG_STRIDE;
    const f4 zf = {0.f, 0.f, 0.f, 0.f};
    Ctx c;
#define COMMON_SETUP() \
    c.uf = opaque(ub + 32 * la + 16 * lg); \
    c.ut = opaque(ub + 8 * lane); \
    c.wt = opaque(sb + W_OFF + c4 * W_STRIDE + 8 * lane); \
    c.fa = opaque(fgb + F_REL + (CS + 2) * ((la + 3) & 3) - 2 * la + 16 * lg + 2); \
    c.ga = opaque(fgb + CS * (la & 3) - 4 * (la & 12) + 16 * lg); \
    c.bd = opaque(sb + B_OFF + 16 * lane);
    unsigned pl, pa, pr, pi, ldh, dkh, uo;
    const u4 z4 = {0u, 0u, 0u, 0u};
    const unsigned za = opaque(sb + FG_OFF + tid * 16);
    if (r == 3) {
        const int lt = tid & 255;
        ldsw<u4>(za + 12288, z4);
        ldsw<u4>(za + 16384 - 960, z4);
        const unsigned zj = min((unsigned)lt >> 2, 39u);
        const unsigned z1 = opaque(U_OFF + zj * 16), z2 = opaque(U_OFF + zj * 1024 + (528 - 33 * 1024)), z3 = opaque(W_OFF + (zj - 38) * 16);
        ldsw<u4>(sb + (lt & 3) * (zj < 38 ? U_STRIDE : W_STRIDE) + (zj < 34 ? z1 : (zj < 38 ? z2 : z3)), z4);
        SB();
        const unsigned idx = min(lt * 20 + xi, 4095);
        const unsigned poff = opaque((idx >> 1) * 1280 + (bx & 7) * 160 + (idx & 1) * 156);
        uo = opaque(lt * 2560 + grp * 8);
        KILL(pa); KILL(pr); KILL(pi); KILL(ldh); KILL(dkh);
        asm volatile("s_waitcnt lgkmcnt(0)" :: "s"(ka) : "memory");
        asm volatile("global_load_dword %0, %1, %2" : "=v"(pl) : "v"(poff), "s"(ka[0]));
    } else {
        ldsw<u4>(za, z4); ldsw<u4>(za + 12288, z4);
        const unsigned vc = opaque(2 * ch), vo = opaque(2 * (ch * NN + r * 64 + lane));
        if (w < 6) {
            const unsigned pat = (unsigned)((w < 4 ? 0x3F801F00001F00E0ull : 0xC07FC0ull) >> (16 * c4)) & 0xffffu;
            const unsigned val = w == 5 ? 0xffffu : 0x3F80u;
            const unsigned by = (pat << la >> (8 * lg)) & 0xffu;
            const unsigned e = by | (by << 15);
            u4 bq;
#pragma unroll
            for (int m = 0; m < 4; m++) bq[m] = __umul24((e >> (2 * m)) & 0x10001u, val);
            ldsw<u4>(za + (B_OFF - FG_OFF), bq);
        }
        ldh = opaque(0u); pl = opaque(0u); pa = opaque(0u); pr = opaque(0u); pi = opaque(0u); dkh = opaque(0u);
        SB();
        asm volatile("s_waitcnt lgkmcnt(0)" :: "s"(ka) : "memory");
        asm volatile("global_load_short_d16_hi %0, %1, %2" : "+v"(ldh) : "v"(vc), "s"(ka[5]));
        asm volatile("global_load_short_d16_hi %0, %1, %2" : "+v"(pl) : "v"(vo), "s"(ka[1]));
        asm volatile("global_load_short_d16_hi %0, %1, %2" : "+v"(pa) : "v"(vo), "s"(ka[2]));
        asm volatile("global_load_short_d16_hi %0, %1, %2" : "+v"(pr) : "v"(vo), "s"(ka[3]));
        asm volatile("global_load_short_d16_hi %0, %1, %2" : "+v"(pi) : "v"(vo), "s"(ka[4]));
        asm volatile("global_load_short_d16_hi %0, %1, %2" : "+v"(dkh) : "v"(vc), "s"(ka[6]));
        KILL(uo);
    }
    STAMP(1);
    asm volatile("s_barrier" ::: "memory");
    STAMP(2);
    SB();
    if (opaques(r) == 3) {
        __builtin_amdgcn_s_setprio(3);
        u2 ua[2], uc[2], ud[2];
#define ULOAD(x) \
        asm volatile("global_load_dwordx2 %0, %1, %2" : "=v"(x[0]) : "v"(uo), "s"(ka[0])); \
        asm volatile("global_load_dwordx2 %0, %1, %2 offset:1280" : "=v"(x[1]) : "v"(uo), "s"(ka[0])); \
        uo += 512 * 1280;
#define UW(cc, h, val) asm volatile("ds_write_addtid_b32 %0 offset:%1" :: "v"(val), "i"((cc) * U_STRIDE + 1024 * (h)) : "memory")
#define UPUT(x, h, n) \
        asm volatile("s_waitcnt vmcnt(" #n ")" : "+v"(x[0]), "+v"(x[1])); \
        UW(0, h, __builtin_amdgcn_perm(x[1][0], x[0][0], 0x05040100u)); \
        UW(1, h, __builtin_amdgcn_perm(x[1][0], x[0][0], 0x07060302u)); \
        UW(2, h, __builtin_amdgcn_perm(x[1][1], x[0][1], 0x05040100u)); \
        UW(3, h, __builtin_amdgcn_perm(x[1][1], x[0][1], 0x07060302u)); \
        arrive4(sw4, 1u << (12 + 3 * (h))); SB();
        ULOAD(ua);
        ULOAD(uc);
        SB();
        c.uf = opaque(ub + 32 * la + 16 * lg);
        c.sw = opaque(sb + MISC_OFF + 4 * c4);
        c.bd = opaque(sb + B_OFF + 16 * lane);
        const unsigned sw4 = opaque(sb + MISC_OFF + 4 * lane);
        const unsigned ww = opaque(sb + W_OFF + 32 + c4 * W_STRIDE + 32 * la + 8 * lg);
        const u4 B1 = BAND(0);
        asm volatile("s_mov_b32 m0, %0\n s_nop 0" :: "s"(sb + U_OFF + 532 + 256 * c4) : "memory");
        SB();
        ULOAD(ud);
        STAMP(3);
        UPUT(ua, 0, 4);
        asm volatile("" :: "v"(pl), "v"(cpf));
        STAMP(4);
        ULOAD(ua); SB();
        UPUT(uc, 1, 4);
        waitm(c.sw, S_U(0)); wbuild<0>(c, B1, ww); arrive(c.sw, S_W01); SB();
        UPUT(ud, 2, 2);
        STAMP(5);
        waitm(c.sw, S_U(1)); wbuild<2>(c, B1, ww); arrive(c.sw, S_W23); SB();
        UPUT(ua, 3, 0);
        STAMP(6);
        waitm(c.sw, S_U(2)); wbuild<4>(c, B1, ww); arrive(c.sw, S_W45); SB();
        waitm(c.sw, S_U(3)); wbuild<6>(c, B1, ww); arrive(c.sw, S_W6);
        STAMP(7);
        FINISH();
        KILL(cpf); KILL(pl); KILL(pa); KILL(pr); KILL(pi); KILL(ldh); KILL(dkh);
    }
    {
        __builtin_amdgcn_s_setprio(3);
        const unsigned tb = sb + w * T_STRIDE;
        const unsigned tr = opaque(tb + 288 * la + 16 * lg);
        const unsigned pw = opaque(sb + P_OFF + c4 * 6144 + 16 * lane);
        const unsigned pw0 = opaque(pw + (r >> 1) * 1024), pw2 = opaque(pw + (r & 1) * 1024);
        unsigned wk, wpt, st;
        c.sw = opaque(sb + MISC_OFF + 4 * c4);
        if (r == 1) {
            const unsigned u0 = __builtin_amdgcn_perm(0x6D492D48u, 0x6C092C08u, la & 7), u1 = __builtin_amdgcn_perm(0x24406401u, 0x25654100u, la & 7);
            wk = opaque(fgb + 8 * ((la < 8 ? u0 : u1) & 0xffu) + 16 * lg);
            const unsigned wa = opaque((CS + 2) * (lane & 3) + F_REL + 54 - 2 * (la >> 2)), wb = opaque(CS * (lane & 3) + ((lane & 3) == 3 ? 80 : 66 + 2 * (lane & 3)));
            wpt = opaque(fgb + (la < 12 ? wa : wb));
            KILL(st); KILL(c.uf); KILL(c.ut); KILL(c.wt); KILL(c.fa); KILL(c.ga); KILL(c.bd);
        } else {
            COMMON_SETUP();
            st = opaque(tb + 16 * c4 + 8 * la + 128 * lg + 64 * (lg >> 1));
            const unsigned u0 = __builtin_amdgcn_perm(0x80889098u, 0x5D1D5C1Cu, la & 7), u1 = __builtin_amdgcn_perm(0u, 0x81899199u, la & 3);
            const unsigned w2 = opaque(8 * ((la < 8 ? u0 : u1) & 0xffu) + 16 * lg), w0 = opaque(F_REL + 56 + 32 * la + 8 * lg);
            wk = opaque(fgb + (r == 0 ? w0 : w2));
            KILL(wpt);
        }
        const unsigned cnt = opaque(sb + MISC_OFF + 16);
        asm volatile("s_mov_b32 m0, %0\n s_nop 0" :: "s"(tb) : "memory");
        STAMP(3);
        asm volatile("s_waitcnt vmcnt(0)" : "+v"(ldh), "+v"(pl), "+v"(pa), "+v"(pr), "+v"(pi), "+v"(dkh) : "v"(cpf));
        const float LOG2E = 1.4426950408889634f;
        const float dt = __uint_as_float(pkbf(0.f, __builtin_amdgcn_exp2f(__uint_as_float(ldh) * LOG2E)));
        const float arp = __uint_as_float(pkbf(0.f, __builtin_amdgcn_exp2f(__uint_as_float(pl) * LOG2E)));
        const float m1 = arp * (-dt * LOG2E);
        const float th = __builtin_amdgcn_fractf(__uint_as_float(pa) * (dt * 0.15915494309189535f));
        const float mg = __builtin_amdgcn_exp2f(m1);
        f2 z = (f2){__builtin_amdgcn_cosf(th), __builtin_amdgcn_sinf(th)} * mg;
        STAMP(4);
        unsigned mx;
        asm("v_max_f32 %0, |%1|, |%2|\n s_nop 1" : "=v"(mx) : "v"(pr), "v"(pi));
#define MXS(ctl, rm) mx = max(mx, (unsigned)__builtin_amdgcn_update_dpp(0, (int)mx, ctl, rm, 0xf, true))
        const h2 e1 = pkh(z); MXS(0x111, 0xf); z = csq(z); SB();
        const h2 e2 = pkh(z); MXS(0x112, 0xf); z = csq(z); SB();
        const h2 e4 = pkh(z); MXS(0x114, 0xf); z = csq(z); SB();
        const h2 e8 = pkh(z);
        const h2 one = {(_Float16)1.f, (_Float16)0.f};
        h2 t0, t1, t2, t3;
        t0 = cmul(e2, e1); MXS(0x118, 0xf); t1 = cmul(e4, e1); TW(15, one); TW(14, e1); SB();
        const h2 y6 = cmul(e4, e2); MXS(0x142, 0xa); t2 = cmul(e8, e1); TW(13, e2); TW(11, e4); TW(7, e8); SB();
        const h2 y10 = cmul(e8, e2); MXS(0x143, 0xc); const h2 y12 = cmul(e8, e4); TW(12, t0); TW(10, t1); SB();
        const float th2 = th * 16.f;
        const float mg2 = __builtin_amdgcn_exp2f(m1 * 16.f);
        t0 = cmul(y6, e1); const h2 y14 = cmul(e8, y6); TW(9, y6); TW(6, t2); SB();
        z = (f2){__builtin_amdgcn_cosf(th2), __builtin_amdgcn_sinf(th2)} * mg2;
        const unsigned ex = max((unsigned)__builtin_amdgcn_readlane(mx, 63) >> 23, 16u);
        const float sc = __uint_as_float((268u - ex) << 23);
        const float ks = dt * __uint_as_float((ex - 13u) << 23);
        t1 = cmul(y10, e1); t2 = cmul(y12, e1); TW(5, y10); TW(3, y12); SB();
        t3 = cmul(y14, e1);
        TW(8, t0); TW(1, y14); SB();
        const h2 x0 = pkh((f2){__uint_as_float(pr), -__uint_as_float(pi)} * sc);
        const h2 e16 = pkh(z); z = csq(z);
        TW(4, t1); TW(2, t2); TW(0, t3);
        u4 yf[4];
#pragma unroll
        for (int s = 0; s < 4; s++) yf[s] = ldsr<u4>(tr + 64 * s);
        const h2 e32 = pkh(z); z = csq(z);
        SB();
        const h2 x1 = cmulc(x0, e16), x2 = cmulc(x0, e32);
        const h2 e64 = pkh(z); z = csq(z);
        TW(15, x0); SB();
        const h2 x3 = cmulc(x1, e32), x4 = cmulc(x0, e64);
        const h2 e128 = pkh(z); SB();
        const h2 x5 = cmulc(x1, e64), x6 = cmulc(x2, e64); TW(14, x1); TW(13, x2); SB();
        const h2 x7 = cmulc(x3, e64); t0 = cmulc(x0, e128); TW(12, x3); TW(11, x4); SB();
        t1 = cmulc(x1, e128); t2 = cmulc(x2, e128); TW(10, x5); TW(9, x6); SB();
        t3 = cmulc(x3, e128); h2 t4 = cmulc(x4, e128); TW(8, x7); TW(7, t0); SB();
        t0 = cmulc(x5, e128); h2 t5 = cmulc(x6, e128); TW(6, t1); TW(5, t2); SB();
        t1 = cmulc(x7, e128); TW(4, t3); TW(3, t4); SB();
        z = csq(z); TW(2, t0); TW(1, t5); SB();
        TW(0, t1);
        u4 xf[4];
#pragma unroll
        for (int s = 0; s < 4; s++) xf[s] = ldsr<u4>(tr + 64 * s);
        const h2 e256 = pkh(z); z = csq(z);
        const h2 p272 = cmul(e256, e16); t0 = cmul(e256, e4);
        SB();
        t1 = cmul(e256, y6); const h2 p264 = cmul(e256, e8); SB();
        f4 acc0 = zf, acc1 = zf, acc2 = zf;
#pragma unroll
        for (int s = 0; s < 4; s++) acc0 = mfh(yf[s], xf[s], acc0);
        t2 = cmul(e256, y10); t3 = cmul(e256, y12); TW(7, p272); TW(1, t0); SB();
        t4 = cmul(e256, y14); t5 = cmul(p272, e2); TW(2, t1); TW(3, p264); SB();
        const h2 e512 = pkh(z); z = csq(z);
        acc0 *= ks;
        STAMP(5);
        asm volatile("s_cmp_eq_u32 %0, 0\n s_cbranch_scc1 .Lp0_%=\n ds_write_b128 %1, %2\n s_mov_b64 exec, 1\n ds_add_u32 %3, %4\n s_mov_b64 exec, -1\n"
                     ".Lp0_%=:\n s_cmp_eq_u32 %0, 2\n s_cbranch_scc1 .Lp1_%=\n s_setprio 2\n .Lp1_%=:"
                     :: "s"(r), "v"(pw0), "v"(acc0), "v"(c.sw), "v"(1u) : "scc", "memory");
        SB();
        const h2 p520 = cmul(e512, e8), p528 = cmul(e512, e16); TW(4, t2); TW(5, t3); SB();
        const h2 p516 = cmul(e512, e4), p524 = cmul(e512, y12); TW(6, t4); TW(0, t5); SB();
        const h2 e1024 = pkh(z);
        t4 = cmul(e256, e2); TW(12, p520); TW(8, p528); TW(9, e256); SB();
        const h2 p776 = cmul(e256, p520), p784 = cmul(e256, p528); TW(15, p516); TW(11, p524); TW(10, t4);
        SB();
#pragma unroll
        for (int s = 0; s < 4; s++) yf[s] = ldsr<u4>(tr + 64 * s);
        t0 = cmul(e256, p516); t1 = cmul(e256, p524);
        t2 = cmul(e1024, e8); t3 = cmul(e1024, p264); TW(1, p776); TW(3, p784); SB();
#pragma unroll
        for (int s = 0; s < 4; s++) acc1 = mfh(xf[s], yf[s], acc1);
        t4 = cmul(e1024, p520); t5 = cmul(e1024, p776); TW(0, t0); TW(2, t1); SB();
        t0 = cmul(e1024, e16); t1 = cmul(e1024, p272); TW(4, t2); TW(5, t3); SB();
        t2 = cmul(e1024, p528); t3 = cmul(e1024, p784); TW(6, t4); TW(7, t5); SB();
        TW(8, t0); TW(9, t1); TW(10, t2); TW(11, t3);
        SB();
#pragma unroll
        for (int s = 0; s < 4; s++) yf[s] = ldsr<u4>(tr + 64 * s);
        acc1 *= ks;
        SB();
        if (r != 1) { ldsw<f4>(pw0 + 2048, acc1); arrive(c.sw, 4u); }
        KSTAMP(8);
        SB();
#pragma unroll
        for (int s = 0; s < 4; s++) acc2 = mfh(xf[s], yf[s], acc2);
        acc2 *= ks;
        if (r == 2) {
            waitm(c.sw, S_C2);
            f4 kk = acc2 + ldsr<f4>(pw + 4096) + ldsr<f4>(pw + 5120);
            const unsigned q01 = pkbf(kk[0], kk[1]), q23 = pkbf(kk[2], kk[3]);
            if (la < 12) put4g(wk, q01, q23);
            const unsigned k1024 = (__builtin_amdgcn_readlane(q01, 3) & 0xffffu) * 0x10001u;
            arrive(c.sw, S_G2);
            __builtin_amdgcn_s_setprio(1);
            STAMP(6);
            f4 a[4] = {zf, zf, zf, zf};
            u4 p[5], v[5], k[4];
            {
                const u4 B3 = BAND(3), B4 = BAND(4), BF = and4(BAND(5), k1024);
#pragma unroll
                for (int d = 0; d < 4; d++) k[d] = KH(4 + d);
                waits(c.sw, S_U(0) | S_U(1));
                u4 q[4], n[4];
#pragma unroll
                for (int S = 0; S < 4; S++) { p[S] = ufrag(c.uf, S, 18); q[S] = ufrag(c.uf, S, 26); }
#pragma unroll
                for (int S = 0; S < 4; S++) n[S] = ufrag(c.uf, S, 10);
                SB();
#pragma unroll
                for (int S = 0; S < 4; S++) v[S] = bx2(p[S], B3, q[S], B4);
#pragma unroll
                for (int t = 0; t < 4; t++) a[t] = mfb(BF, n[t], a[t]);
            }
            const u4 B2a = BAND(1), B2b = BAND(2);
            SB();
#pragma unroll
            for (int S = 0; S < 4; S++)
#pragma unroll
                for (int t = S; t < 4; t++) a[t] = mfb(v[S], k[t - S], a[t]);
#pragma unroll
            for (int S = 0; S < 4; S++) v[S] = bx2(p[S], B2b, p[S], B2a);
            waits(c.sw, S_G1 | S_W45);
            STAMP(7);
            p[4] = ufrag(c.uf, 4, 18);
            const u4 kd3 = KD(3), kd4 = KD(4);
            u4 vc[6];
#pragma unroll
            for (int S = 0; S < 6; S++) vc[S] = VC(S);
#pragma unroll
            for (int d = 0; d < 4; d++) k[d] = KC(4 - d);
            SB();
            v[4] = bx2(p[4], B2b, p[4], B2a);
#pragma unroll
            for (int t = 0; t < 4; t++) a[t] = mfb(v[t], kd4, a[t]);
#pragma unroll
            for (int t = 0; t < 4; t++) a[t] = mfb(v[t + 1], kd3, a[t]);
            SB();
            u4 k1[4], k2;
#pragma unroll
            for (int S = 0; S < 4; S++) v[S] = VA(2 + S);
            k2 = KA(2); k1[0] = KA(1);
#pragma unroll
            for (int d = 0; d < 4; d++)
#pragma unroll
                for (int t = 0; t < 4; t++) if (t + d < 6) a[t] = mfb(vc[t + d], k[d], a[t]);
            k1[2] = k[3];
            SB();
#pragma unroll
            for (int S = 0; S < 4; S++) p[S] = VB(2 + S);
            k[0] = KB(2); k1[1] = KB(1);
#pragma unroll
            for (int t = 0; t < 4; t++) a[t] = mfb(v[t], k2, a[t]);
#pragma unroll
            for (int t = 0; t < 3; t++) a[t] = mfb(v[t + 1], k1[0], a[t]);
            SB();
#pragma unroll
            for (int S = 0; S < 4; S++) v[S] = VD(2 + S);
            k2 = KD(2); k1[3] = KD(1);
#pragma unroll
            for (int t = 0; t < 4; t++) a[t] = mfb(p[t], k[0], a[t]);
#pragma unroll
            for (int t = 0; t < 3; t++) a[t] = mfb(p[t + 1], k1[1], a[t]);
            SB();
            waitm(c.sw, S_F);
            u4 F[9];
#pragma unroll
            for (int i = 0; i < 9; i++) F[i] = ffrag(c.fa, i);
#pragma unroll
            for (int t = 0; t < 4; t++) a[t] = mfb(v[t], k2, a[t]);
#pragma unroll
            for (int t = 0; t < 3; t++) a[t] = mfb(v[t + 1], k1[3], a[t]);
            STAMP(8);
            near9<4>(c, F, a[0], a[1]);
            STAGE(2, a[0], a[1]);
            STAMP(9);
            waitm(c.sw, S_W6);
            near9<6>(c, F, a[2], a[3]);
            {
                const u4 va = VA(6), vb = VB(6), vc6 = VC(6), vd = VD(6);
                a[3] = mfb(va, k1[0], a[3]); a[3] = mfb(vb, k1[1], a[3]); a[3] = mfb(vc6, k1[2], a[3]); a[3] = mfb(vd, k1[3], a[3]);
            }
            STAGE(3, a[2], a[3]);
            STAMP(10);
            FINISH();
            KILL(acc0); KILL(acc1); KILL(acc2); KILL(wk); KILL(wpt); KILL(dkh);
        }
        ldsw<f4>(pw2 + 4096, acc2);
        arrive(c.sw, 16u);
        __builtin_amdgcn_s_setprio(3);
        KSTAMP(7);
        if (r == 1) {
            waitm(c.sw, S_C1);
            f4 kk = acc1 + ldsr<f4>(pw + 2048) + ldsr<f4>(pw + 3072);
            unsigned q01 = pkbf(kk[0], kk[1]), q23 = pkbf(kk[2], kk[3]);
            const unsigned k256 = __builtin_amdgcn_readlane(q23, 57) >> 16;
            const unsigned k258 = __builtin_amdgcn_readlane(q23, 58) >> 16;
            const unsigned k512 = __builtin_amdgcn_readlane(q01, 7);
            q01 = __builtin_amdgcn_update_dpp(q01, q01, 0xC0, 0xf, 0xc, false);
            q23 = __builtin_amdgcn_update_dpp(q23, q23, 0xC0, 0xf, 0xc, false);
            put4g(wk, q01, q23);
            CFENCE();
            ldsw<unsigned short>(wpt, (unsigned short)(la < 8 ? k256 : (la < 12 ? k258 : k512)));
            arrive(c.sw, S_G1);
            STAMP(6);
            const unsigned ea = opaque(sb + ((lane >> 2) & 3) * (T_STRIDE + 16) + 8 * lg + 128 * (lane & 3) + 32 * (lane & 2) + 64 * (c4 & 1) + 576 * (c4 >> 1));
            auto ro = __builtin_amdgcn_make_buffer_rsrc((void*)ka[7], (short)0, 2048 * 1280, 0x00020000);
            const unsigned vo = opaque((lane + 128 * c4) * 1280 + grp * 8);
#pragma unroll
            for (int pp = 0; pp < 4; pp++) {
                waits(cnt, 4u << (3 * pp));
                const u2 v0 = ldtr(ea + STG(pp)), v1 = ldtr(ea + STG(pp) + 32);
                __builtin_amdgcn_raw_buffer_store_b64(v0, ro, vo, 512 * pp * 1280, 0);
                __builtin_amdgcn_raw_buffer_store_b64(v1, ro, vo, (512 * pp + 64) * 1280, 0);
                STAMP(7 + pp);
            }
            FINISH();
            KILL(acc0); KILL(wk); KILL(dkh);
        }
        {
            waitm(c.sw, S_C0);
            f4 kk = acc0 + ldsr<f4>(pw) + ldsr<f4>(pw + 1024);
            kk[3] += lane == 63 ? __uint_as_float(dkh) : 0.f;
            put4(wk, pkbf(kk[0], kk[1]), pkbf(kk[2], kk[3]));
            arrive(c.sw, S_F);
            __builtin_amdgcn_s_setprio(XPRIO);
            STAMP(6);
            f4 a[4] = {zf, zf, zf, zf};
            u4 F[8];
#pragma unroll
            for (int i = 0; i < 8; i++) F[i] = ffrag(c.fa, i);
            waits(c.sw, S_U(0));
            STAMP(7);
            near8<0>(c, F, a[0], a[1]);
            waits(c.sw, S_U(1));
            near8<2>(c, F, a[2], a[3]);
            u4 n[4];
#pragma unroll
            for (int t = 0; t < 4; t++) n[t] = ufrag(c.uf, t, 266);
            const u4 va0 = VA(0), vb0 = VB(0);
            waits(c.sw, S_G1 | S_W01);
            STAMP(8);
            __builtin_amdgcn_s_setprio(2);
            const u4 F8 = ffrag(c.fa, 8), ka1 = KA(1), kb1 = KB(1), vc0 = VC(0), kc1 = KC(1), vd0 = VD(0), kd1 = KD(1);
            SB();
            const u4 va1 = VA(1), vb1 = VB(1), vc1 = VC(1), vd1 = VD(1), ka2 = KA(2), kb2 = KB(2), kc2 = KC(2), kd2 = KD(2);
            a[1] = mfb(va0, ka1, a[1]); a[0] = mfb(F8, n[0], a[0]); a[1] = mfb(vb0, kb1, a[1]); a[1] = mfb(F8, n[1], a[1]);
            a[1] = mfb(vc0, kc1, a[1]); a[1] = mfb(vd0, kd1, a[1]);
            a[2] = mfb(F8, n[2], a[2]); a[3] = mfb(F8, n[3], a[3]);
            STAGE(0, a[0], a[1]);
            STAMP(9);
            const u4 B2a = BAND(1), B2b = BAND(2), p0 = ufrag(c.uf, 0, 18), va2 = VA(2), vb2 = VB(2);
            a[2] = mfb(va1, ka1, a[2]); a[3] = mfb(va1, ka2, a[3]); a[2] = mfb(va0, ka2, a[2]);
            a[2] = mfb(vb1, kb1, a[2]); a[3] = mfb(vb1, kb2, a[3]); a[2] = mfb(vb0, kb2, a[2]);
            waitm(c.sw, S_G2 | S_W23);
            const u4 vc2 = VC(2), vd2 = VD(2), kc3 = KC(3), kd3 = KD(3);
            const u4 w0 = bx2(p0, B2b, p0, B2a);
            a[2] = mfb(vc1, kc1, a[2]); a[3] = mfb(vc1, kc2, a[3]); a[2] = mfb(vc0, kc2, a[2]);
            a[2] = mfb(vd1, kd1, a[2]); a[3] = mfb(vd1, kd2, a[3]); a[2] = mfb(vd0, kd2, a[2]);
            a[3] = mfb(va2, ka1, a[3]); a[3] = mfb(vb2, kb1, a[3]); a[3] = mfb(vc2, kc1, a[3]); a[3] = mfb(vd2, kd1, a[3]);
            a[3] = mfb(vc0, kc3, a[3]); a[3] = mfb(w0, kd3, a[3]);
            STAGE(1, a[2], a[3]);
            STAMP(10);
            FINISH();
        }
    }
}

extern "C" void kernel_launch(void* const* d_in, const int* in_sizes, int n_in, void* d_out, int out_size, void* d_ws, size_t ws_size,
                              hipStream_t stream) {
    s4d_kernel<<<160, 1024, 0, stream>>>((const unsigned short*)d_in[0], (const unsigned short*)d_in[1], (const unsigned short*)d_in[2],
                                         (const unsigned short*)d_in[3], (const unsigned short*)d_in[4], (const unsigned short*)d_in[5],
                                         (const unsigned short*)d_in[6], (unsigned short*)d_out
    );
}
```

```cpp
#include <hip/hip_runtime.h>
#include <stdint.h>

typedef _Float16 h2 __attribute__((ext_vector_type(2)));
typedef _Float16 h8 __attribute__((ext_vector_type(8)));
typedef __bf16 b2 __attribute__((ext_vector_type(2)));
typedef __bf16 b8 __attribute__((ext_vector_type(8)));
typedef float f2 __attribute__((ext_vector_type(2)));
typedef float f4 __attribute__((ext_vector_type(4)));
typedef unsigned u2 __attribute__((ext_vector_type(2)));
typedef unsigned u4 __attribute__((ext_vector_type(4)));
typedef unsigned long long l8 __attribute__((ext_vector_type(8)));
typedef short s4 __attribute__((ext_vector_type(4)));
#define AS3 __attribute__((address_space(3)))
#define DEV __device__ __forceinline__
#define SB() __builtin_amdgcn_sched_barrier(0)
#define KILL(x) asm volatile("" : "=v"(x))
#define CFENCE() asm volatile("" ::: "memory")

#define NN 192
#define XPRIO 1
#define CODE_BYTES 12288

#define T_STRIDE 4608
#define U_OFF 55296
#define U_STRIDE 4640
#define FG_OFF 73856
#define CS 1984
#define FG_STRIDE (4 * CS)
#define F_REL 1360
#define MISC_OFF (FG_OFF + 4 * FG_STRIDE)
#define W_OFF (MISC_OFF + 64)
#define W_STRIDE 3648
#define P_OFF (W_OFF + 4 * W_STRIDE)
#define B_OFF (P_OFF + 4 * 6144)
#define LDS_BYTES (B_OFF + 6144)
#define S_C0 2u
#define S_C1 8u
#define S_C2 32u
#define S_F 64u
#define S_G1 128u
#define S_G2 256u
#define S_W01 512u
#define S_W23 1024u
#define S_W45 2048u
#define S_U(h) (4u << (12 + 3 * (h)))
#define S_W6 (1u << 24)

#define STAMP(i)

#define KSTAMP(i)
template <class T> DEV T ldsr(unsigned a) { return *(const AS3 T*)(uintptr_t)(a); }
template <class T> DEV T ldsrv(unsigned a) { return *(const volatile AS3 T*)(uintptr_t)(a); }
template <class T> DEV void ldsw(unsigned a, T v) { *(AS3 T*)(uintptr_t)(a) = v; }
DEV u2 ldtr(unsigned a) { s4 v = __builtin_amdgcn_ds_read_tr16_b64_v4i16((AS3 s4*)(uintptr_t)(a)); return __builtin_bit_cast(u2, v); }
DEV int opaques(int x) { asm volatile("" : "+s"(x)); return x; }
DEV unsigned opaque(unsigned x) { asm volatile("" : "+v"(x)); return x; }

DEV unsigned pkbf(float a, float b) { f2 v = {a, b}; return __builtin_bit_cast(unsigned, __builtin_convertvector(v, b2)); }
DEV h2 cmul(h2 h, h2 e) { h2 t = h * (h2){e.x, e.x}; return __builtin_elementwise_fma((h2){-h.y, h.x}, (h2){e.y, e.y}, t); }
DEV h2 cmulc(h2 h, h2 e) { h2 t = h * (h2){e.x, e.x}; return __builtin_elementwise_fma((h2){h.y, -h.x}, (h2){e.y, e.y}, t); }
DEV f4 mfb(u4 a, u4 b, f4 c) { return __builtin_amdgcn_mfma_f32_16x16x32_bf16(__builtin_bit_cast(b8, a), __builtin_bit_cast(b8, b), c, 0, 0, 0); }
DEV f4 mfh(u4 a, u4 b, f4 c) { return __builtin_amdgcn_mfma_f32_16x16x32_f16(__builtin_bit_cast(h8, a), __builtin_bit_cast(h8, b), c, 0, 0, 0); }
DEV u4 cat(u2 a, u2 b) { return (u4){a.x, a.y, b.x, b.y}; }

#define TW(row, val) asm volatile("ds_write_addtid_b32 %0 offset:%1" :: "v"(val), "i"((row) * 288) : "memory")

DEV f2 csq(f2 z) {
    f2 t, r;
    asm("v_pk_mul_f32 %0, %1, %1 op_sel_hi:[0,1]" : "=v"(t) : "v"(z));
    asm("v_pk_fma_f32 %0, %1, %1, %2 op_sel:[1,1,0] op_sel_hi:[0,1,1] neg_lo:[1,0,0]" : "=v"(r) : "v"(z), "v"(t));
    return r;
}
DEV h2 pkh(f2 v) { return __builtin_convertvector(v, h2); }

DEV void arrive(unsigned a, unsigned v) {
    asm volatile("s_mov_b64 exec, 1\n ds_add_u32 %0, %1\n s_mov_b64 exec, -1" :: "v"(a), "v"(v) : "memory");
}
DEV void arrive4(unsigned a, unsigned v) {
    asm volatile("s_mov_b64 exec, 15\n ds_add_u32 %0, %1\n s_mov_b64 exec, -1" :: "v"(a), "v"(v) : "memory");
}
DEV void waitm(unsigned a, unsigned m) {
    CFENCE();
    while ((__builtin_amdgcn_readfirstlane(ldsrv<unsigned>(a)) & m) != m) {}
    CFENCE();
}

DEV void waits(unsigned a, unsigned m) {
    CFENCE();
    while ((__builtin_amdgcn_readfirstlane(ldsrv<unsigned>(a)) & m) != m) __builtin_amdgcn_s_sleep(1);
    CFENCE();
}
DEV h2 oph(h2 x) { asm volatile("" : "+v"(x)); return x; }

DEV void put4(unsigned a, unsigned p01, unsigned p23) {
    unsigned p12 = __builtin_amdgcn_alignbit(p23, p01, 16);
    ldsw<u2>(a, (u2){p01, p23});
    ldsw<unsigned short>(a + CS + 2, (unsigned short)p01);
    ldsw<unsigned>(a + CS + 4, p12);
    ldsw<unsigned short>(a + CS + 8, (unsigned short)(p23 >> 16));
    ldsw<unsigned>(a + 2 * CS + 4, p01);
    ldsw<unsigned>(a + 2 * CS + 8, p23);
    ldsw<unsigned short>(a + 3 * CS + 6, (unsigned short)p01);
    ldsw<unsigned>(a + 3 * CS + 8, p12);
    ldsw<unsigned short>(a + 3 * CS + 12, (unsigned short)(p23 >> 16));
}

DEV void put4g(unsigned a, unsigned p01, unsigned p23) {
    unsigned p12 = __builtin_amdgcn_alignbit(p23, p01, 16);
    ldsw<u2>(a + 3 * CS + 16, (u2){p01, p23});
    ldsw<unsigned short>(a + 2, (unsigned short)p01);
    ldsw<unsigned>(a + 4, p12);
    ldsw<unsigned short>(a + 16, (unsigned short)(p23 >> 16));
    ldsw<unsigned>(a + CS + 4, p01);
    ldsw<unsigned>(a + CS + 16, p23);
    ldsw<unsigned short>(a + 2 * CS + 6, (unsigned short)p01);
    ldsw<unsigned>(a + 2 * CS + 16, p12);
    ldsw<unsigned short>(a + 2 * CS + 20, (unsigned short)(p23 >> 16));
}

struct Ctx {
    unsigned uf;
    unsigned ut;
    unsigned wt;
    unsigned fa, ga;
    unsigned sw;
    unsigned bd;
};

DEV u4 ufrag(unsigned uf, int S, int s0) { return ldsr<u4>(uf + 512 * S + 2 * (266 - s0)); }
DEV u4 gfrag(unsigned ga, int ytop, int d) { return ldsr<u4>(ga + 4 * (ytop + 1 - 16 * d)); }
DEV u4 ffrag(unsigned fa, int i) { return cat(ldsrv<u2>(fa + 544 - 64 * i), ldsrv<u2>(fa + 552 - 64 * i)); }
DEV u2 cv4(f4 d) { return (u2){pkbf(d[0], d[1]), pkbf(d[2], d[3])}; }
DEV u4 utr(const Ctx& c, int S, int p1, int p2) { return cat(ldtr(c.ut + 512 * S + 2 * (266 - p1)), ldtr(c.ut + 512 * S + 2 * (266 - p2))); }
DEV u4 wtr(const Ctx& c, int S, int p1, int p2) { return cat(ldtr(c.wt + 512 * S + 2 * (20 - p1)), ldtr(c.wt + 512 * S + 2 * (20 - p2))); }
#define KA(d) gfrag(c.ga, 47, d)
#define KB(d) gfrag(c.ga, 175, d)
#define KC(d) gfrag(c.ga, 119, d)
#define KD(d) gfrag(c.ga, 247, d)
#define KH(d) gfrag(c.ga, 383, d)
#define VA(S) utr(c, S, 18, 6)
#define VB(S) utr(c, S, 10, 14)
#define VC(S) wtr(c, S, 4, 12)
#define VD(S) wtr(c, S, 8, 16)
#define BAND(i) ldsr<u4>(c.bd + 1024 * (i))
DEV u4 bx2(u4 a, u4 b1, u4 a2, u4 b2) {
    const f4 z = {0.f, 0.f, 0.f, 0.f};
    return cat(cv4(mfb(a, b1, z)), cv4(mfb(a2, b2, z)));
}
DEV u4 and4(u4 m, unsigned v) { return (u4){m[0] & v, m[1] & v, m[2] & v, m[3] & v}; }

template <int R> DEV void near8(const Ctx& c, const u4* F, f4& a0, f4& a1) {
    u4 f0[8], f1[8];
#pragma unroll
    for (int i = 0; i < 3; i++) { f0[i] = ufrag(c.uf, R, 10 + 32 * i); f1[i] = ufrag(c.uf, R + 1, 10 + 32 * i); }
    SB();
#pragma unroll
    for (int i = 3; i < 6; i++) { f0[i] = ufrag(c.uf, R, 10 + 32 * i); f1[i] = ufrag(c.uf, R + 1, 10 + 32 * i); }
#pragma unroll
    for (int i = 0; i < 3; i++) { a0 = mfb(F[i], f0[i], a0); a1 = mfb(F[i], f1[i], a1); }
    SB();
#pragma unroll
    for (int i = 6; i < 8; i++) { f0[i] = ufrag(c.uf, R, 10 + 32 * i); f1[i] = ufrag(c.uf, R + 1, 10 + 32 * i); }
#pragma unroll
    for (int i = 3; i < 6; i++) { a0 = mfb(F[i], f0[i], a0); a1 = mfb(F[i], f1[i], a1); }
    SB();
#pragma unroll
    for (int i = 6; i < 8; i++) { a0 = mfb(F[i], f0[i], a0); a1 = mfb(F[i], f1[i], a1); }
}
template <int R> DEV void near9(const Ctx& c, const u4* F, f4& a0, f4& a1) {
    u4 f0[9], f1[8];
#pragma unroll
    for (int i = 0; i < 3; i++) { f0[i] = ufrag(c.uf, R, 10 + 32 * i); f1[i] = ufrag(c.uf, R + 1, 10 + 32 * i); }
    SB();
#pragma unroll
    for (int i = 3; i < 6; i++) { f0[i] = ufrag(c.uf, R, 10 + 32 * i); f1[i] = ufrag(c.uf, R + 1, 10 + 32 * i); }
#pragma unroll
    for (int i = 0; i < 3; i++) { a0 = mfb(F[i], f0[i], a0); a1 = mfb(F[i], f1[i], a1); }
    a1 = mfb(F[8], f0[0], a1);
    SB();
#pragma unroll
    for (int i = 6; i < 9; i++) { f0[i] = ufrag(c.uf, R, 10 + 32 * i); if (i < 8) f1[i] = ufrag(c.uf, R + 1, 10 + 32 * i); }
#pragma unroll
    for (int i = 3; i < 6; i++) { a0 = mfb(F[i], f0[i], a0); a1 = mfb(F[i], f1[i], a1); }
    SB();
#pragma unroll
    for (int i = 6; i < 9; i++) { a0 = mfb(F[i], f0[i], a0); if (i < 8) a1 = mfb(F[i], f1[i], a1); }
}
template <int S> DEV void wbuild(const Ctx& c, u4 B1, unsigned ww) {
    const f4 z = {0.f, 0.f, 0.f, 0.f};
    const u4 n0 = ufrag(c.uf, S, 10);
    u4 n1;
    if (S < 6) n1 = ufrag(c.uf, S + 1, 10);
    ldsw<u2>(ww + 512 * S, cv4(mfb(B1, n0, z)));
    if (S < 6) ldsw<u2>(ww + 512 * (S + 1), cv4(mfb(B1, n1, z)));
}

#define PROF_OUT()
#define STG(pp) (((pp) >> 1) * 8 * T_STRIDE + ((pp) & 1) * 2048)
#define STAGE(pp, x0, x1) do { ldsw<u2>(st + ((pp) & 1) * 2048, cv4(x0)); ldsw<u2>(st + ((pp) & 1) * 2048 + 576, cv4(x1)); \
    arrive(cnt, 1u << (3 * (pp))); } while (0)
#define FINISH() do { PROF_OUT(); asm volatile("s_endpgm" ::: "memory"); } while (0)

__global__ __launch_bounds__(1024, 4) void s4d_kernel(const unsigned short* __restrict__ u, const unsigned short* __restrict__ lar,
                                                      const unsigned short* __restrict__ aim, const unsigned short* __restrict__ cre,
                                                      const unsigned short* __restrict__ cim, const unsigned short* __restrict__ ldt,
                                                      const unsigned short* __restrict__ dsk, unsigned short* __restrict__ out
) {
    __shared__ __attribute__((aligned(16))) char smem[LDS_BYTES];
    const unsigned sb = (unsigned)(uintptr_t)(AS3 char*)smem;
    STAMP(0);
    const int tid = threadIdx.x;
    l8 ka;
    unsigned cpf;
    {
        unsigned long long pc;
        asm volatile("s_load_dwordx16 %0, %2, 0x0\n"
                     "s_getpc_b64 %1" : "=&s"(ka), "=&s"(pc) : "s"(__builtin_amdgcn_kernarg_segment_ptr()));
        asm volatile("v_lshlrev_b32 %0, 7, %2\n"
                     "v_min_u32 %0, %3, %0\n"
                     "global_load_dword %0, %0, %1"
                     : "=&v"(cpf) : "s"(pc), "v"(tid), "i"(CODE_BYTES));
    }
    SB();
    const int lane = tid & 63;
    const int w = __builtin_amdgcn_readfirstlane(tid >> 6);
    const int c4 = w & 3, r = w >> 2;
    const int bx = blockIdx.x;
    const int xi = bx >> 3;
    const int grp = (bx & 7) * 20 + xi;
    const int ch = grp * 4 + c4;
    const int la = lane & 15, lg = lane >> 4;

    const unsigned ub = sb + U_OFF + c4 * U_STRIDE;
    const unsigned fgb = sb + FG_OFF + c4 * FG_STRIDE;
    const f4 zf = {0.f, 0.f, 0.f, 0.f};
    Ctx c;
#define COMMON_SETUP() \
    c.uf = opaque(ub + 32 * la + 16 * lg); \
    c.ut = opaque(ub + 8 * lane); \
    c.wt = opaque(sb + W_OFF + c4 * W_STRIDE + 8 * lane); \
    c.fa = opaque(fgb + F_REL + (CS + 2) * ((la + 3) & 3) - 2 * la + 16 * lg + 2); \
    c.ga = opaque(fgb + CS * (la & 3) - 4 * (la & 12) + 16 * lg); \
    c.bd = opaque(sb + B_OFF + 16 * lane);
    unsigned pl, pa, pr, pi, ldh, dkh, uo;
    const u4 z4 = {0u, 0u, 0u, 0u};
    const unsigned za = opaque(sb + FG_OFF + tid * 16);
    if (r == 3) {
        const int lt = tid & 255;
        ldsw<u4>(za + 12288, z4);
        ldsw<u4>(za + 16384 - 960, z4);
        const unsigned zj = min((unsigned)lt >> 2, 39u);
        const unsigned z1 = opaque(U_OFF + zj * 16), z2 = opaque(U_OFF + zj * 1024 + (528 - 33 * 1024)), z3 = opaque(W_OFF + (zj - 38) * 16);
        ldsw<u4>(sb + (lt & 3) * (zj < 38 ? U_STRIDE : W_STRIDE) + (zj < 34 ? z1 : (zj < 38 ? z2 : z3)), z4);
        SB();
        const unsigned idx = min(lt * 20 + xi, 4095);
        const unsigned poff = opaque((idx >> 1) * 1280 + (bx & 7) * 160 + (idx & 1) * 156);
        uo = opaque(lt * 2560 + grp * 8);
        KILL(pa); KILL(pr); KILL(pi); KILL(ldh); KILL(dkh);
        asm volatile("s_waitcnt lgkmcnt(0)" :: "s"(ka) : "memory");
        asm volatile("global_load_dword %0, %1, %2" : "=v"(pl) : "v"(poff), "s"(ka[0]));
    } else {
        ldsw<u4>(za, z4); ldsw<u4>(za + 12288, z4);
        const unsigned vc = opaque(2 * ch), vo = opaque(2 * (ch * NN + r * 64 + lane));
        if (w < 6) {
            const unsigned pat = (unsigned)((w < 4 ? 0x3F801F00001F00E0ull : 0xC07FC0ull) >> (16 * c4)) & 0xffffu;
            const unsigned val = w == 5 ? 0xffffu : 0x3F80u;
            const unsigned by = (pat << la >> (8 * lg)) & 0xffu;
            const unsigned e = by | (by << 15);
            u4 bq;
#pragma unroll
            for (int m = 0; m < 4; m++) bq[m] = __umul24((e >> (2 * m)) & 0x10001u, val);
            ldsw<u4>(za + (B_OFF - FG_OFF), bq);
        }
        ldh = opaque(0u); pl = opaque(0u); pa = opaque(0u); pr = opaque(0u); pi = opaque(0u); dkh = opaque(0u);
        SB();
        asm volatile("s_waitcnt lgkmcnt(0)" :: "s"(ka) : "memory");
        asm volatile("global_load_short_d16_hi %0, %1, %2" : "+v"(ldh) : "v"(vc), "s"(ka[5]));
        asm volatile("global_load_short_d16_hi %0, %1, %2" : "+v"(pl) : "v"(vo), "s"(ka[1]));
        asm volatile("global_load_short_d16_hi %0, %1, %2" : "+v"(pa) : "v"(vo), "s"(ka[2]));
        asm volatile("global_load_short_d16_hi %0, %1, %2" : "+v"(pr) : "v"(vo), "s"(ka[3]));
        asm volatile("global_load_short_d16_hi %0, %1, %2" : "+v"(pi) : "v"(vo), "s"(ka[4]));
        asm volatile("global_load_short_d16_hi %0, %1, %2" : "+v"(dkh) : "v"(vc), "s"(ka[6]));
        KILL(uo);
    }
    STAMP(1);
    asm volatile("s_barrier" ::: "memory");
    STAMP(2);
    SB();
    if (opaques(r) == 3) {
        __builtin_amdgcn_s_setprio(3);
        u2 ua[2], uc[2], ud[2];
#define ULOAD(x) \
        asm volatile("global_load_dwordx2 %0, %1, %2" : "=v"(x[0]) : "v"(uo), "s"(ka[0])); \
        asm volatile("global_load_dwordx2 %0, %1, %2 offset:1280" : "=v"(x[1]) : "v"(uo), "s"(ka[0])); \
        uo += 512 * 1280;
#define UW(cc, h, val) asm volatile("ds_write_addtid_b32 %0 offset:%1" :: "v"(val), "i"((cc) * U_STRIDE + 1024 * (h)) : "memory")
#define UPUT(x, h, n) \
        asm volatile("s_waitcnt vmcnt(" #n ")" : "+v"(x[0]), "+v"(x[1])); \
        UW(0, h, __builtin_amdgcn_perm(x[1][0], x[0][0], 0x05040100u)); \
        UW(1, h, __builtin_amdgcn_perm(x[1][0], x[0][0], 0x07060302u)); \
        UW(2, h, __builtin_amdgcn_perm(x[1][1], x[0][1], 0x05040100u)); \
        UW(3, h, __builtin_amdgcn_perm(x[1][1], x[0][1], 0x07060302u)); \
        arrive4(sw4, 1u << (12 + 3 * (h))); SB();
        ULOAD(ua);
        ULOAD(uc);
        SB();
        c.uf = opaque(ub + 32 * la + 16 * lg);
        c.sw = opaque(sb + MISC_OFF + 4 * c4);
        c.bd = opaque(sb + B_OFF + 16 * lane);
        const unsigned sw4 = opaque(sb + MISC_OFF + 4 * lane);
        const unsigned ww = opaque(sb + W_OFF + 32 + c4 * W_STRIDE + 32 * la + 8 * lg);
        const u4 B1 = BAND(0);
        asm volatile("s_mov_b32 m0, %0\n s_nop 0" :: "s"(sb + U_OFF + 532 + 256 * c4) : "memory");
        SB();
        ULOAD(ud);
        STAMP(3);
        UPUT(ua, 0, 4);
        asm volatile("" :: "v"(pl), "v"(cpf));
        STAMP(4);
        ULOAD(ua); SB();
        UPUT(uc, 1, 4);
        waitm(c.sw, S_U(0)); wbuild<0>(c, B1, ww); arrive(c.sw, S_W01); SB();
        UPUT(ud, 2, 2);
        STAMP(5);
        waitm(c.sw, S_U(1)); wbuild<2>(c, B1, ww); arrive(c.sw, S_W23); SB();
        UPUT(ua, 3, 0);
        STAMP(6);
        waitm(c.sw, S_U(2)); wbuild<4>(c, B1, ww); arrive(c.sw, S_W45); SB();
        waitm(c.sw, S_U(3)); wbuild<6>(c, B1, ww); arrive(c.sw, S_W6);
        STAMP(7);
        FINISH();
        KILL(cpf); KILL(pl); KILL(pa); KILL(pr); KILL(pi); KILL(ldh); KILL(dkh);
    }
    {
        __builtin_amdgcn_s_setprio(3);
        const unsigned tb = sb + w * T_STRIDE;
        const unsigned tr = opaque(tb + 288 * la + 16 * lg);
        const unsigned pw = opaque(sb + P_OFF + c4 * 6144 + 16 * lane);
        const unsigned pw0 = opaque(pw + (r >> 1) * 1024), pw2 = opaque(pw + (r & 1) * 1024);
        unsigned wk, wpt, st;
        c.sw = opaque(sb + MISC_OFF + 4 * c4);
        if (r == 1) {
            const unsigned u0 = __builtin_amdgcn_perm(0x6D492D48u, 0x6C092C08u, la & 7), u1 = __builtin_amdgcn_perm(0x24406401u, 0x25654100u, la & 7);
            wk = opaque(fgb + 8 * ((la < 8 ? u0 : u1) & 0xffu) + 16 * lg);
            const unsigned wa = opaque((CS + 2) * (lane & 3) + F_REL + 54 - 2 * (la >> 2)), wb = opaque(CS * (lane & 3) + ((lane & 3) == 3 ? 80 : 66 + 2 * (lane & 3)));
            wpt = opaque(fgb + (la < 12 ? wa : wb));
            KILL(st); KILL(c.uf); KILL(c.ut); KILL(c.wt); KILL(c.fa); KILL(c.ga); KILL(c.bd);
        } else {
            COMMON_SETUP();
            st = opaque(tb + 16 * c4 + 8 * la + 128 * lg + 64 * (lg >> 1));
            const unsigned u0 = __builtin_amdgcn_perm(0x80889098u, 0x5D1D5C1Cu, la & 7), u1 = __builtin_amdgcn_perm(0u, 0x81899199u, la & 3);
            const unsigned w2 = opaque(8 * ((la < 8 ? u0 : u1) & 0xffu) + 16 * lg), w0 = opaque(F_REL + 56 + 32 * la + 8 * lg);
            wk = opaque(fgb + (r == 0 ? w0 : w2));
            KILL(wpt);
        }
        const unsigned cnt = opaque(sb + MISC_OFF + 16);
        asm volatile("s_mov_b32 m0, %0\n s_nop 0" :: "s"(tb) : "memory");
        STAMP(3);
        asm volatile("s_waitcnt vmcnt(0)" : "+v"(ldh), "+v"(pl), "+v"(pa), "+v"(pr), "+v"(pi), "+v"(dkh) : "v"(cpf));
        const float LOG2E = 1.4426950408889634f;
        const float dt = __uint_as_float(pkbf(0.f, __builtin_amdgcn_exp2f(__uint_as_float(ldh) * LOG2E)));
        const float arp = __uint_as_float(pkbf(0.f, __builtin_amdgcn_exp2f(__uint_as_float(pl) * LOG2E)));
        const float m1 = arp * (-dt * LOG2E);
        const float th = __builtin_amdgcn_fractf(__uint_as_float(pa) * (dt * 0.15915494309189535f));
        const float mg = __builtin_amdgcn_exp2f(m1);
        f2 z = (f2){__builtin_amdgcn_cosf(th), __builtin_amdgcn_sinf(th)} * mg;
        STAMP(4);
        unsigned mx;
        asm("v_max_f32 %0, |%1|, |%2|\n s_nop 1" : "=v"(mx) : "v"(pr), "v"(pi));
#define MXS(ctl, rm) mx = max(mx, (unsigned)__builtin_amdgcn_update_dpp(0, (int)mx, ctl, rm, 0xf, true))
        const h2 e1 = pkh(z); MXS(0x111, 0xf); z = csq(z); SB();
        const h2 e2 = pkh(z); MXS(0x112, 0xf); z = csq(z); SB();
        const h2 e4 = pkh(z); MXS(0x114, 0xf); z = csq(z); SB();
        const h2 e8 = pkh(z);
        const h2 one = {(_Float16)1.f, (_Float16)0.f};
        h2 t0, t1, t2, t3;
        t0 = cmul(e2, e1); MXS(0x118, 0xf); t1 = cmul(e4, e1); TW(15, one); TW(14, e1); SB();
        const h2 y6 = cmul(e4, e2); MXS(0x142, 0xa); t2 = cmul(e8, e1); TW(13, e2); TW(11, e4); TW(7, e8); SB();
        const h2 y10 = cmul(e8, e2); MXS(0x143, 0xc); const h2 y12 = cmul(e8, e4); TW(12, t0); TW(10, t1); SB();
        const float th2 = th * 16.f;
        const float mg2 = __builtin_amdgcn_exp2f(m1 * 16.f);
        t0 = cmul(y6, e1); const h2 y14 = cmul(e8, y6); TW(9, y6); TW(6, t2); SB();
        z = (f2){__builtin_amdgcn_cosf(th2), __builtin_amdgcn_sinf(th2)} * mg2;
        const unsigned ex = max((unsigned)__builtin_amdgcn_readlane(mx, 63) >> 23, 16u);
        const float sc = __uint_as_float((268u - ex) << 23);
        const float ks = dt * __uint_as_float((ex - 13u) << 23);
        t1 = cmul(y10, e1); t2 = cmul(y12, e1); TW(5, y10); TW(3, y12); SB();
        t3 = cmul(y14, e1);
        TW(8, t0); TW(1, y14); SB();
        const h2 x0 = pkh((f2){__uint_as_float(pr), -__uint_as_float(pi)} * sc);
        const h2 e16 = pkh(z); z = csq(z);
        TW(4, t1); TW(2, t2); TW(0, t3);
        u4 yf[4];
#pragma unroll
        for (int s = 0; s < 4; s++) yf[s] = ldsr<u4>(tr + 64 * s);
        const h2 e32 = pkh(z); z = csq(z);
        SB();
        const h2 x1 = cmulc(x0, e16), x2 = cmulc(x0, e32);
        const h2 e64 = pkh(z); z = csq(z);
        TW(15, x0); SB();
        const h2 x3 = cmulc(x1, e32), x4 = cmulc(x0, e64);
        const h2 e128 = pkh(z); SB();
        const h2 x5 = cmulc(x1, e64), x6 = cmulc(x2, e64); TW(14, x1); TW(13, x2); SB();
        const h2 x7 = cmulc(x3, e64); t0 = cmulc(x0, e128); TW(12, x3); TW(11, x4); SB();
        t1 = cmulc(x1, e128); t2 = cmulc(x2, e128); TW(10, x5); TW(9, x6); SB();
        t3 = cmulc(x3, e128); h2 t4 = cmulc(x4, e128); TW(8, x7); TW(7, t0); SB();
        t0 = cmulc(x5, e128); h2 t5 = cmulc(x6, e128); TW(6, t1); TW(5, t2); SB();
        t1 = cmulc(x7, e128); TW(4, t3); TW(3, t4); SB();
        z = csq(z); TW(2, t0); TW(1, t5); SB();
        TW(0, t1);
        u4 xf[4];
#pragma unroll
        for (int s = 0; s < 4; s++) xf[s] = ldsr<u4>(tr + 64 * s);
        const h2 e256 = pkh(z); z = csq(z);
        const h2 p272 = cmul(e256, e16); t0 = cmul(e256, e4);
        SB();
        t1 = cmul(e256, y6); const h2 p264 = cmul(e256, e8); SB();
        f4 acc0 = zf, acc1 = zf, acc2 = zf;
#pragma unroll
        for (int s = 0; s < 4; s++) acc0 = mfh(yf[s], xf[s], acc0);
        t2 = cmul(e256, y10); t3 = cmul(e256, y12); TW(7, p272); TW(1, t0); SB();
        t4 = cmul(e256, y14); t5 = cmul(p272, e2); TW(2, t1); TW(3, p264); SB();
        const h2 e512 = pkh(z); z = csq(z);
        acc0 *= ks;
        STAMP(5);
        asm volatile("s_cmp_eq_u32 %0, 0\n s_cbranch_scc1 .Lp0_%=\n ds_write_b128 %1, %2\n s_mov_b64 exec, 1\n ds_add_u32 %3, %4\n s_mov_b64 exec, -1\n"
                     ".Lp0_%=:\n s_cmp_eq_u32 %0, 2\n s_cbranch_scc1 .Lp1_%=\n s_setprio 2\n .Lp1_%=:"
                     :: "s"(r), "v"(pw0), "v"(acc0), "v"(c.sw), "v"(1u) : "scc", "memory");
        SB();
        const h2 p520 = cmul(e512, e8), p528 = cmul(e512, e16); TW(4, t2); TW(5, t3); SB();
        const h2 p516 = cmul(e512, e4), p524 = cmul(e512, y12); TW(6, t4); TW(0, t5); SB();
        const h2 e1024 = pkh(z);
        t4 = cmul(e256, e2); TW(12, p520); TW(8, p528); TW(9, e256); SB();
        const h2 p776 = cmul(e256, p520), p784 = cmul(e256, p528); TW(15, p516); TW(11, p524); TW(10, t4);
        SB();
#pragma unroll
        for (int s = 0; s < 4; s++) yf[s] = ldsr<u4>(tr + 64 * s);
        t0 = cmul(e256, p516); t1 = cmul(e256, p524);
        t2 = cmul(e1024, e8); t3 = cmul(e1024, p264); TW(1, p776); TW(3, p784); SB();
#pragma unroll
        for (int s = 0; s < 4; s++) acc1 = mfh(xf[s], yf[s], acc1);
        t4 = cmul(e1024, p520); t5 = cmul(e1024, p776); TW(0, t0); TW(2, t1); SB();
        t0 = cmul(e1024, e16); t1 = cmul(e1024, p272); TW(4, t2); TW(5, t3); SB();
        t2 = cmul(e1024, p528); t3 = cmul(e1024, p784); TW(6, t4); TW(7, t5); SB();
        TW(8, t0); TW(9, t1); TW(10, t2); TW(11, t3);
        SB();
#pragma unroll
        for (int s = 0; s < 4; s++) yf[s] = ldsr<u4>(tr + 64 * s);
        acc1 *= ks;
        SB();
        if (r != 1) { ldsw<f4>(pw0 + 2048, acc1); arrive(c.sw, 4u); }
        KSTAMP(8);
        SB();
#pragma unroll
        for (int s = 0; s < 4; s++) acc2 = mfh(xf[s], yf[s], acc2);
        acc2 *= ks;
        if (r == 2) {
            waitm(c.sw, S_C2);
            f4 kk = acc2 + ldsr<f4>(pw + 4096) + ldsr<f4>(pw + 5120);
            const unsigned q01 = pkbf(kk[0], kk[1]), q23 = pkbf(kk[2], kk[3]);
            if (la < 12) put4g(wk, q01, q23);
            const unsigned k1024 = (__builtin_amdgcn_readlane(q01, 3) & 0xffffu) * 0x10001u;
            arrive(c.sw, S_G2);
            __builtin_amdgcn_s_setprio(1);
            STAMP(6);
            f4 a[4] = {zf, zf, zf, zf};
            u4 p[5], v[5], k[4];
            {
                const u4 B3 = BAND(3), B4 = BAND(4), BF = and4(BAND(5), k1024);
#pragma unroll
                for (int d = 0; d < 4; d++) k[d] = KH(4 + d);
                waits(c.sw, S_U(0) | S_U(1));
                u4 q[4], n[4];
#pragma unroll
                for (int S = 0; S < 4; S++) { p[S] = ufrag(c.uf, S, 18); q[S] = ufrag(c.uf, S, 26); }
#pragma unroll
                for (int S = 0; S < 4; S++) n[S] = ufrag(c.uf, S, 10);
                SB();
#pragma unroll
                for (int S = 0; S < 4; S++) v[S] = bx2(p[S], B3, q[S], B4);
#pragma unroll
                for (int t = 0; t < 4; t++) a[t] = mfb(BF, n[t], a[t]);
            }
            const u4 B2a = BAND(1), B2b = BAND(2);
            SB();
#pragma unroll
            for (int S = 0; S < 4; S++)
#pragma unroll
                for (int t = S; t < 4; t++) a[t] = mfb(v[S], k[t - S], a[t]);
#pragma unroll
            for (int S = 0; S < 4; S++) v[S] = bx2(p[S], B2b, p[S], B2a);
            waits(c.sw, S_G1 | S_W45);
            STAMP(7);
            p[4] = ufrag(c.uf, 4, 18);
            const u4 kd3 = KD(3), kd4 = KD(4);
            u4 vc[6];
#pragma unroll
            for (int S = 0; S < 6; S++) vc[S] = VC(S);
#pragma unroll
            for (int d = 0; d < 4; d++) k[d] = KC(4 - d);
            SB();
            v[4] = bx2(p[4], B2b, p[4], B2a);
#pragma unroll
            for (int t = 0; t < 4; t++) a[t] = mfb(v[t], kd4, a[t]);
#pragma unroll
            for (int t = 0; t < 4; t++) a[t] = mfb(v[t + 1], kd3, a[t]);
            SB();
            u4 k1[4], k2;
#pragma unroll
            for (int S = 0; S < 4; S++) v[S] = VA(2 + S);
            k2 = KA(2); k1[0] = KA(1);
#pragma unroll
            for (int d = 0; d < 4; d++)
#pragma unroll
                for (int t = 0; t < 4; t++) if (t + d < 6) a[t] = mfb(vc[t + d], k[d], a[t]);
            k1[2] = k[3];
            SB();
#pragma unroll
            for (int S = 0; S < 4; S++) p[S] = VB(2 + S);
            k[0] = KB(2); k1[1] = KB(1);
#pragma unroll
            for (int t = 0; t < 4; t++) a[t] = mfb(v[t], k2, a[t]);
#pragma unroll
            for (int t = 0; t < 3; t++) a[t] = mfb(v[t + 1], k1[0], a[t]);
            SB();
#pragma unroll
            for (int S = 0; S < 4; S++) v[S] = VD(2 + S);
            k2 = KD(2); k1[3] = KD(1);
#pragma unroll
            for (int t = 0; t < 4; t++) a[t] = mfb(p[t], k[0], a[t]);
#pragma unroll
            for (int t = 0; t < 3; t++) a[t] = mfb(p[t + 1], k1[1], a[t]);
            SB();
            waitm(c.sw, S_F);
            u4 F[9];
#pragma unroll
            for (int i = 0; i < 9; i++) F[i] = ffrag(c.fa, i);
#pragma unroll
            for (int t = 0; t < 4; t++) a[t] = mfb(v[t], k2, a[t]);
#pragma unroll
            for (int t = 0; t < 3; t++) a[t] = mfb(v[t + 1], k1[3], a[t]);
            STAMP(8);
            near9<4>(c, F, a[0], a[1]);
            STAGE(2, a[0], a[1]);
            STAMP(9);
            waitm(c.sw, S_W6);
            near9<6>(c, F, a[2], a[3]);
            {
                const u4 va = VA(6), vb = VB(6), vc6 = VC(6), vd = VD(6);
                a[3] = mfb(va, k1[0], a[3]); a[3] = mfb(vb, k1[1], a[3]); a[3] = mfb(vc6, k1[2], a[3]); a[3] = mfb(vd, k1[3], a[3]);
            }
            STAGE(3, a[2], a[3]);
            STAMP(10);
            FINISH();
            KILL(acc0); KILL(acc1); KILL(acc2); KILL(wk); KILL(wpt); KILL(dkh);
        }
        ldsw<f4>(pw2 + 4096, acc2);
        arrive(c.sw, 16u);
        __builtin_amdgcn_s_setprio(3);
        KSTAMP(7);
        if (r == 1) {
            waitm(c.sw, S_C1);
            f4 kk = acc1 + ldsr<f4>(pw + 2048) + ldsr<f4>(pw + 3072);
            unsigned q01 = pkbf(kk[0], kk[1]), q23 = pkbf(kk[2], kk[3]);
            const unsigned k256 = __builtin_amdgcn_readlane(q23, 57) >> 16;
            const unsigned k258 = __builtin_amdgcn_readlane(q23, 58) >> 16;
            const unsigned k512 = __builtin_amdgcn_readlane(q01, 7);
            q01 = __builtin_amdgcn_update_dpp(q01, q01, 0xC0, 0xf, 0xc, false);
            q23 = __builtin_amdgcn_update_dpp(q23, q23, 0xC0, 0xf, 0xc, false);
            put4g(wk, q01, q23);
            CFENCE();
            ldsw<unsigned short>(wpt, (unsigned short)(la < 8 ? k256 : (la < 12 ? k258 : k512)));
            arrive(c.sw, S_G1);
            STAMP(6);
            const unsigned ea = opaque(sb + ((lane >> 2) & 3) * (T_STRIDE + 16) + 8 * lg + 128 * (lane & 3) + 32 * (lane & 2) + 64 * (c4 & 1) + 576 * (c4 >> 1));
            auto ro = __builtin_amdgcn_make_buffer_rsrc((void*)ka[7], (short)0, 2048 * 1280, 0x00020000);
            const unsigned vo = opaque((lane + 128 * c4) * 1280 + grp * 8);
#pragma unroll
            for (int pp = 0; pp < 4; pp++) {
                waits(cnt, 4u << (3 * pp));
                const u2 v0 = ldtr(ea + STG(pp)), v1 = ldtr(ea + STG(pp) + 32);
                __builtin_amdgcn_raw_buffer_store_b64(v0, ro, vo, 512 * pp * 1280, 2);
                __builtin_amdgcn_raw_buffer_store_b64(v1, ro, vo, (512 * pp + 64) * 1280, 2);
                STAMP(7 + pp);
            }
            FINISH();
            KILL(acc0); KILL(wk); KILL(dkh);
        }
        {
            waitm(c.sw, S_C0);
            f4 kk = acc0 + ldsr<f4>(pw) + ldsr<f4>(pw + 1024);
            kk[3] += lane == 63 ? __uint_as_float(dkh) : 0.f;
            put4(wk, pkbf(kk[0], kk[1]), pkbf(kk[2], kk[3]));
            arrive(c.sw, S_F);
            __builtin_amdgcn_s_setprio(XPRIO);
            STAMP(6);
            f4 a[4] = {zf, zf, zf, zf};
            u4 F[8];
#pragma unroll
            for (int i = 0; i < 8; i++) F[i] = ffrag(c.fa, i);
            waits(c.sw, S_U(0));
            STAMP(7);
            near8<0>(c, F, a[0], a[1]);
            waits(c.sw, S_U(1));
            near8<2>(c, F, a[2], a[3]);
            u4 n[4];
#pragma unroll
            for (int t = 0; t < 4; t++) n[t] = ufrag(c.uf, t, 266);
            const u4 va0 = VA(0), vb0 = VB(0);
            waits(c.sw, S_G1 | S_W01);
            STAMP(8);
            __builtin_amdgcn_s_setprio(2);
            const u4 F8 = ffrag(c.fa, 8), ka1 = KA(1), kb1 = KB(1), vc0 = VC(0), kc1 = KC(1), vd0 = VD(0), kd1 = KD(1);
            SB();
            const u4 va1 = VA(1), vb1 = VB(1), vc1 = VC(1), vd1 = VD(1), ka2 = KA(2), kb2 = KB(2), kc2 = KC(2), kd2 = KD(2);
            a[1] = mfb(va0, ka1, a[1]); a[0] = mfb(F8, n[0], a[0]); a[1] = mfb(vb0, kb1, a[1]); a[1] = mfb(F8, n[1], a[1]);
            a[1] = mfb(vc0, kc1, a[1]); a[1] = mfb(vd0, kd1, a[1]);
            a[2] = mfb(F8, n[2], a[2]); a[3] = mfb(F8, n[3], a[3]);
            STAGE(0, a[0], a[1]);
            STAMP(9);
            const u4 B2a = BAND(1), B2b = BAND(2), p0 = ufrag(c.uf, 0, 18), va2 = VA(2), vb2 = VB(2);
            a[2] = mfb(va1, ka1, a[2]); a[3] = mfb(va1, ka2, a[3]); a[2] = mfb(va0, ka2, a[2]);
            a[2] = mfb(vb1, kb1, a[2]); a[3] = mfb(vb1, kb2, a[3]); a[2] = mfb(vb0, kb2, a[2]);
            waitm(c.sw, S_G2 | S_W23);
            const u4 vc2 = VC(2), vd2 = VD(2), kc3 = KC(3), kd3 = KD(3);
            const u4 w0 = bx2(p0, B2b, p0, B2a);
            a[2] = mfb(vc1, kc1, a[2]); a[3] = mfb(vc1, kc2, a[3]); a[2] = mfb(vc0, kc2, a[2]);
            a[2] = mfb(vd1, kd1, a[2]); a[3] = mfb(vd1, kd2, a[3]); a[2] = mfb(vd0, kd2, a[2]);
            a[3] = mfb(va2, ka1, a[3]); a[3] = mfb(vb2, kb1, a[3]); a[3] = mfb(vc2, kc1, a[3]); a[3] = mfb(vd2, kd1, a[3]);
            a[3] = mfb(vc0, kc3, a[3]); a[3] = mfb(w0, kd3, a[3]);
            STAGE(1, a[2], a[3]);
            STAMP(10);
            FINISH();
        }
    }
}

extern "C" void kernel_launch(void* const* d_in, const int* in_sizes, int n_in, void* d_out, int out_size, void* d_ws, size_t ws_size,
                              hipStream_t stream) {
    s4d_kernel<<<160, 1024, 0, stream>>>((const unsigned short*)d_in[0], (const unsigned short*)d_in[1], (const unsigned short*)d_in[2],
                                         (const unsigned short*)d_in[3], (const unsigned short*)d_in[4], (const unsigned short*)d_in[5],
                                         (const unsigned short*)d_in[6], (unsigned short*)d_out
    );
}
```

```cpp
#define WBP 1
#include <hip/hip_runtime.h>
#include <stdint.h>

typedef _Float16 h2 __attribute__((ext_vector_type(2)));
typedef _Float16 h8 __attribute__((ext_vector_type(8)));
typedef __bf16 b2 __attribute__((ext_vector_type(2)));
typedef __bf16 b8 __attribute__((ext_vector_type(8)));
typedef float f2 __attribute__((ext_vector_type(2)));
typedef float f4 __attribute__((ext_vector_type(4)));
typedef unsigned u2 __attribute__((ext_vector_type(2)));
typedef unsigned u4 __attribute__((ext_vector_type(4)));
typedef unsigned long long l8 __attribute__((ext_vector_type(8)));
typedef short s4 __attribute__((ext_vector_type(4)));
#define AS3 __attribute__((address_space(3)))
#define DEV __device__ __forceinline__
#define SB() __builtin_amdgcn_sched_barrier(0)
#define KILL(x) asm volatile("" : "=v"(x))
#define CFENCE() asm volatile("" ::: "memory")

#define NN 192
#define XPRIO 1
#define CODE_BYTES 12288

#define T_STRIDE 4608
#define U_OFF 55296
#define U_STRIDE 4640
#define FG_OFF 73856
#define CS 1984
#define FG_STRIDE (4 * CS)
#define F_REL 1360
#define MISC_OFF (FG_OFF + 4 * FG_STRIDE)
#define W_OFF (MISC_OFF + 64)
#define W_STRIDE 3648
#define P_OFF (W_OFF + 4 * W_STRIDE)
#define B_OFF (P_OFF + 4 * 6144)
#define LDS_BYTES (B_OFF + 6144)
#define S_C0 2u
#define S_C1 8u
#define S_C2 32u
#define S_F 64u
#define S_G1 128u
#define S_G2 256u
#define S_W01 512u
#define S_W23 1024u
#define S_W45 2048u
#define S_U(h) (4u << (12 + 3 * (h)))
#define S_W6 (1u << 24)

#define STAMP(i)

#define KSTAMP(i)
template <class T> DEV T ldsr(unsigned a) { return *(const AS3 T*)(uintptr_t)(a); }
template <class T> DEV T ldsrv(unsigned a) { return *(const volatile AS3 T*)(uintptr_t)(a); }
template <class T> DEV void ldsw(unsigned a, T v) { *(AS3 T*)(uintptr_t)(a) = v; }
DEV u2 ldtr(unsigned a) { s4 v = __builtin_amdgcn_ds_read_tr16_b64_v4i16((AS3 s4*)(uintptr_t)(a)); return __builtin_bit_cast(u2, v); }
DEV int opaques(int x) { asm volatile("" : "+s"(x)); return x; }
DEV unsigned opaque(unsigned x) { asm volatile("" : "+v"(x)); return x; }

DEV unsigned pkbf(float a, float b) { f2 v = {a, b}; return __builtin_bit_cast(unsigned, __builtin_convertvector(v, b2)); }
DEV h2 cmul(h2 h, h2 e) { h2 t = h * (h2){e.x, e.x}; return __builtin_elementwise_fma((h2){-h.y, h.x}, (h2){e.y, e.y}, t); }
DEV h2 cmulc(h2 h, h2 e) { h2 t = h * (h2){e.x, e.x}; return __builtin_elementwise_fma((h2){h.y, -h.x}, (h2){e.y, e.y}, t); }
DEV f4 mfb(u4 a, u4 b, f4 c) { return __builtin_amdgcn_mfma_f32_16x16x32_bf16(__builtin_bit_cast(b8, a), __builtin_bit_cast(b8, b), c, 0, 0, 0); }
DEV f4 mfh(u4 a, u4 b, f4 c) { return __builtin_amdgcn_mfma_f32_16x16x32_f16(__builtin_bit_cast(h8, a), __builtin_bit_cast(h8, b), c, 0, 0, 0); }
DEV u4 cat(u2 a, u2 b) { return (u4){a.x, a.y, b.x, b.y}; }

#define TW(row, val) asm volatile("ds_write_addtid_b32 %0 offset:%1" :: "v"(val), "i"((row) * 288) : "memory")

DEV f2 csq(f2 z) {
    f2 t, r;
    asm("v_pk_mul_f32 %0, %1, %1 op_sel_hi:[0,1]" : "=v"(t) : "v"(z));
    asm("v_pk_fma_f32 %0, %1, %1, %2 op_sel:[1,1,0] op_sel_hi:[0,1,1] neg_lo:[1,0,0]" : "=v"(r) : "v"(z), "v"(t));
    return r;
}
DEV h2 pkh(f2 v) { return __builtin_convertvector(v, h2); }

DEV void arrive(unsigned a, unsigned v) {
    asm volatile("s_mov_b64 exec, 1\n ds_add_u32 %0, %1\n s_mov_b64 exec, -1" :: "v"(a), "v"(v) : "memory");
}
DEV void arrive4(unsigned a, unsigned v) {
    asm volatile("s_mov_b64 exec, 15\n ds_add_u32 %0, %1\n s_mov_b64 exec, -1" :: "v"(a), "v"(v) : "memory");
}
DEV void waitm(unsigned a, unsigned m) {
    CFENCE();
    while ((__builtin_amdgcn_readfirstlane(ldsrv<unsigned>(a)) & m) != m) {}
    CFENCE();
}

DEV void waits(unsigned a, unsigned m) {
    CFENCE();
    while ((__builtin_amdgcn_readfirstlane(ldsrv<unsigned>(a)) & m) != m) __builtin_amdgcn_s_sleep(1);
    CFENCE();
}
DEV h2 oph(h2 x) { asm volatile("" : "+v"(x)); return x; }

DEV void put4(unsigned a, unsigned p01, unsigned p23) {
    unsigned p12 = __builtin_amdgcn_alignbit(p23, p01, 16);
    ldsw<u2>(a, (u2){p01, p23});
    ldsw<unsigned short>(a + CS + 2, (unsigned short)p01);
    ldsw<unsigned>(a + CS + 4, p12);
    ldsw<unsigned short>(a + CS + 8, (unsigned short)(p23 >> 16));
    ldsw<unsigned>(a + 2 * CS + 4, p01);
    ldsw<unsigned>(a + 2 * CS + 8, p23);
    ldsw<unsigned short>(a + 3 * CS + 6, (unsigned short)p01);
    ldsw<unsigned>(a + 3 * CS + 8, p12);
    ldsw<unsigned short>(a + 3 * CS + 12, (unsigned short)(p23 >> 16));
}

DEV void put4g(unsigned a, unsigned p01, unsigned p23) {
    unsigned p12 = __builtin_amdgcn_alignbit(p23, p01, 16);
    ldsw<u2>(a + 3 * CS + 16, (u2){p01, p23});
    ldsw<unsigned short>(a + 2, (unsigned short)p01);
    ldsw<unsigned>(a + 4, p12);
    ldsw<unsigned short>(a + 16, (unsigned short)(p23 >> 16));
    ldsw<unsigned>(a + CS + 4, p01);
    ldsw<unsigned>(a + CS + 16, p23);
    ldsw<unsigned short>(a + 2 * CS + 6, (unsigned short)p01);
    ldsw<unsigned>(a + 2 * CS + 16, p12);
    ldsw<unsigned short>(a + 2 * CS + 20, (unsigned short)(p23 >> 16));
}

struct Ctx {
    unsigned uf;
    unsigned ut;
    unsigned wt;
    unsigned fa, ga;
    unsigned sw;
    unsigned bd;
};

DEV u4 ufrag(unsigned uf, int S, int s0) { return ldsr<u4>(uf + 512 * S + 2 * (266 - s0)); }
DEV u4 gfrag(unsigned ga, int ytop, int d) { return ldsr<u4>(ga + 4 * (ytop + 1 - 16 * d)); }
DEV u4 ffrag(unsigned fa, int i) { return cat(ldsrv<u2>(fa + 544 - 64 * i), ldsrv<u2>(fa + 552 - 64 * i)); }
DEV u2 cv4(f4 d) { return (u2){pkbf(d[0], d[1]), pkbf(d[2], d[3])}; }
DEV u4 utr(const Ctx& c, int S, int p1, int p2) { return cat(ldtr(c.ut + 512 * S + 2 * (266 - p1)), ldtr(c.ut + 512 * S + 2 * (266 - p2))); }
DEV u4 wtr(const Ctx& c, int S, int p1, int p2) { return cat(ldtr(c.wt + 512 * S + 2 * (20 - p1)), ldtr(c.wt + 512 * S + 2 * (20 - p2))); }
#define KA(d) gfrag(c.ga, 47, d)
#define KB(d) gfrag(c.ga, 175, d)
#define KC(d) gfrag(c.ga, 119, d)
#define KD(d) gfrag(c.ga, 247, d)
#define KH(d) gfrag(c.ga, 383, d)
#define VA(S) utr(c, S, 18, 6)
#define VB(S) utr(c, S, 10, 14)
#define VC(S) wtr(c, S, 4, 12)
#define VD(S) wtr(c, S, 8, 16)
#define BAND(i) ldsr<u4>(c.bd + 1024 * (i))
DEV u4 bx2(u4 a, u4 b1, u4 a2, u4 b2) {
    const f4 z = {0.f, 0.f, 0.f, 0.f};
    return cat(cv4(mfb(a, b1, z)), cv4(mfb(a2, b2, z)));
}
DEV u4 and4(u4 m, unsigned v) { return (u4){m[0] & v, m[1] & v, m[2] & v, m[3] & v}; }

template <int R> DEV void near8(const Ctx& c, const u4* F, f4& a0, f4& a1) {
    u4 f0[8], f1[8];
#pragma unroll
    for (int i = 0; i < 3; i++) { f0[i] = ufrag(c.uf, R, 10 + 32 * i); f1[i] = ufrag(c.uf, R + 1, 10 + 32 * i); }
    SB();
#pragma unroll
    for (int i = 3; i < 6; i++) { f0[i] = ufrag(c.uf, R, 10 + 32 * i); f1[i] = ufrag(c.uf, R + 1, 10 + 32 * i); }
#pragma unroll
    for (int i = 0; i < 3; i++) { a0 = mfb(F[i], f0[i], a0); a1 = mfb(F[i], f1[i], a1); }
    SB();
#pragma unroll
    for (int i = 6; i < 8; i++) { f0[i] = ufrag(c.uf, R, 10 + 32 * i); f1[i] = ufrag(c.uf, R + 1, 10 + 32 * i); }
#pragma unroll
    for (int i = 3; i < 6; i++) { a0 = mfb(F[i], f0[i], a0); a1 = mfb(F[i], f1[i], a1); }
    SB();
#pragma unroll
    for (int i = 6; i < 8; i++) { a0 = mfb(F[i], f0[i], a0); a1 = mfb(F[i], f1[i], a1); }
}
template <int R> DEV void near9(const Ctx& c, const u4* F, f4& a0, f4& a1) {
    u4 f0[9], f1[8];
#pragma unroll
    for (int i = 0; i < 3; i++) { f0[i] = ufrag(c.uf, R, 10 + 32 * i); f1[i] = ufrag(c.uf, R + 1, 10 + 32 * i); }
    SB();
#pragma unroll
    for (int i = 3; i < 6; i++) { f0[i] = ufrag(c.uf, R, 10 + 32 * i); f1[i] = ufrag(c.uf, R + 1, 10 + 32 * i); }
#pragma unroll
    for (int i = 0; i < 3; i++) { a0 = mfb(F[i], f0[i], a0); a1 = mfb(F[i], f1[i], a1); }
    a1 = mfb(F[8], f0[0], a1);
    SB();
#pragma unroll
    for (int i = 6; i < 9; i++) { f0[i] = ufrag(c.uf, R, 10 + 32 * i); if (i < 8) f1[i] = ufrag(c.uf, R + 1, 10 + 32 * i); }
#pragma unroll
    for (int i = 3; i < 6; i++) { a0 = mfb(F[i], f0[i], a0); a1 = mfb(F[i], f1[i], a1); }
    SB();
#pragma unroll
    for (int i = 6; i < 9; i++) { a0 = mfb(F[i], f0[i], a0); if (i < 8) a1 = mfb(F[i], f1[i], a1); }
}
template <int S> DEV void wbuild(const Ctx& c, u4 B1, unsigned ww) {
    const f4 z = {0.f, 0.f, 0.f, 0.f};
    const u4 n0 = ufrag(c.uf, S, 10);
    u4 n1;
    if (S < 6) n1 = ufrag(c.uf, S + 1, 10);
    ldsw<u2>(ww + 512 * S, cv4(mfb(B1, n0, z)));
    if (S < 6) ldsw<u2>(ww + 512 * (S + 1), cv4(mfb(B1, n1, z)));
}

#define PROF_OUT()
#define STG(pp) (((pp) >> 1) * 8 * T_STRIDE + ((pp) & 1) * 2048)
#define STAGE(pp, x0, x1) do { ldsw<u2>(st + ((pp) & 1) * 2048, cv4(x0)); ldsw<u2>(st + ((pp) & 1) * 2048 + 576, cv4(x1)); \
    arrive(cnt, 1u << (3 * (pp))); } while (0)
#define FINISH() do { PROF_OUT(); asm volatile("s_endpgm" ::: "memory"); } while (0)

__global__ __launch_bounds__(1024, 4) void s4d_kernel(const unsigned short* __restrict__ u, const unsigned short* __restrict__ lar,
                                                      const unsigned short* __restrict__ aim, const unsigned short* __restrict__ cre,
                                                      const unsigned short* __restrict__ cim, const unsigned short* __restrict__ ldt,
                                                      const unsigned short* __restrict__ dsk, unsigned short* __restrict__ out
) {
    __shared__ __attribute__((aligned(16))) char smem[LDS_BYTES];
    const unsigned sb = (unsigned)(uintptr_t)(AS3 char*)smem;
    STAMP(0);
    const int tid = threadIdx.x;
    l8 ka;
    unsigned cpf;
    {
        unsigned long long pc;
        asm volatile("s_load_dwordx16 %0, %2, 0x0\n"
                     "s_getpc_b64 %1" : "=&s"(ka), "=&s"(pc) : "s"(__builtin_amdgcn_kernarg_segment_ptr()));
        asm volatile("v_lshlrev_b32 %0, 7, %2\n"
                     "v_min_u32 %0, %3, %0\n"
                     "global_load_dword %0, %0, %1"
                     : "=&v"(cpf) : "s"(pc), "v"(tid), "i"(CODE_BYTES));
    }
    SB();
    const int lane = tid & 63;
    const int w = __builtin_amdgcn_readfirstlane(tid >> 6);
    const int c4 = w & 3, r = w >> 2;
    const int bx = blockIdx.x;
    const int xi = bx >> 3;
    const int grp = (bx & 7) * 20 + xi;
    const int ch = grp * 4 + c4;
    const int la = lane & 15, lg = lane >> 4;

    const unsigned ub = sb + U_OFF + c4 * U_STRIDE;
    const unsigned fgb = sb + FG_OFF + c4 * FG_STRIDE;
    const f4 zf = {0.f, 0.f, 0.f, 0.f};
    Ctx c;
#define COMMON_SETUP() \
    c.uf = opaque(ub + 32 * la + 16 * lg); \
    c.ut = opaque(ub + 8 * lane); \
    c.wt = opaque(sb + W_OFF + c4 * W_STRIDE + 8 * lane); \
    c.fa = opaque(fgb + F_REL + (CS + 2) * ((la + 3) & 3) - 2 * la + 16 * lg + 2); \
    c.ga = opaque(fgb + CS * (la & 3) - 4 * (la & 12) + 16 * lg); \
    c.bd = opaque(sb + B_OFF + 16 * lane);
    unsigned pl, pa, pr, pi, ldh, dkh, uo;
    const u4 z4 = {0u, 0u, 0u, 0u};
    const unsigned za = opaque(sb + FG_OFF + tid * 16);
    if (r == 3) {
        const int lt = tid & 255;
        ldsw<u4>(za + 12288, z4);
        ldsw<u4>(za + 16384 - 960, z4);
        const unsigned zj = min((unsigned)lt >> 2, 39u);
        const unsigned z1 = opaque(U_OFF + zj * 16), z2 = opaque(U_OFF + zj * 1024 + (528 - 33 * 1024)), z3 = opaque(W_OFF + (zj - 38) * 16);
        ldsw<u4>(sb + (lt & 3) * (zj < 38 ? U_STRIDE : W_STRIDE) + (zj < 34 ? z1 : (zj < 38 ? z2 : z3)), z4);
        SB();
        const unsigned idx = min(lt * 20 + xi, 4095);
        const unsigned poff = opaque((idx >> 1) * 1280 + (bx & 7) * 160 + (idx & 1) * 156);
        uo = opaque(lt * 2560 + grp * 8);
        KILL(pa); KILL(pr); KILL(pi); KILL(ldh); KILL(dkh);
        asm volatile("s_waitcnt lgkmcnt(0)" :: "s"(ka) : "memory");
        asm volatile("global_load_dword %0, %1, %2" : "=v"(pl) : "v"(poff), "s"(ka[0]));
    } else {
        ldsw<u4>(za, z4); ldsw<u4>(za + 12288, z4);
        const unsigned vc = opaque(2 * ch), vo = opaque(2 * (ch * NN + r * 64 + lane));
        if (w < 6) {
            const unsigned pat = (unsigned)((w < 4 ? 0x3F801F00001F00E0ull : 0xC07FC0ull) >> (16 * c4)) & 0xffffu;
            const unsigned val = w == 5 ? 0xffffu : 0x3F80u;
            const unsigned by = (pat << la >> (8 * lg)) & 0xffu;
            const unsigned e = by | (by << 15);
            u4 bq;
#pragma unroll
            for (int m = 0; m < 4; m++) bq[m] = __umul24((e >> (2 * m)) & 0x10001u, val);
            ldsw<u4>(za + (B_OFF - FG_OFF), bq);
        }
        ldh = opaque(0u); pl = opaque(0u); pa = opaque(0u); pr = opaque(0u); pi = opaque(0u); dkh = opaque(0u);
        SB();
        asm volatile("s_waitcnt lgkmcnt(0)" :: "s"(ka) : "memory");
        asm volatile("global_load_short_d16_hi %0, %1, %2" : "+v"(ldh) : "v"(vc), "s"(ka[5]));
        asm volatile("global_load_short_d16_hi %0, %1, %2" : "+v"(pl) : "v"(vo), "s"(ka[1]));
        asm volatile("global_load_short_d16_hi %0, %1, %2" : "+v"(pa) : "v"(vo), "s"(ka[2]));
        asm volatile("global_load_short_d16_hi %0, %1, %2" : "+v"(pr) : "v"(vo), "s"(ka[3]));
        asm volatile("global_load_short_d16_hi %0, %1, %2" : "+v"(pi) : "v"(vo), "s"(ka[4]));
        asm volatile("global_load_short_d16_hi %0, %1, %2" : "+v"(dkh) : "v"(vc), "s"(ka[6]));
        KILL(uo);
    }
    STAMP(1);
    asm volatile("s_barrier" ::: "memory");
    STAMP(2);
    SB();
    if (opaques(r) == 3) {
        __builtin_amdgcn_s_setprio(3);
        u2 ua[2], uc[2], ud[2];
#define ULOAD(x) \
        asm volatile("global_load_dwordx2 %0, %1, %2" : "=v"(x[0]) : "v"(uo), "s"(ka[0])); \
        asm volatile("global_load_dwordx2 %0, %1, %2 offset:1280" : "=v"(x[1]) : "v"(uo), "s"(ka[0])); \
        uo += 512 * 1280;
#define UW(cc, h, val) asm volatile("ds_write_addtid_b32 %0 offset:%1" :: "v"(val), "i"((cc) * U_STRIDE + 1024 * (h)) : "memory")
#define UPUT(x, h, n) \
        asm volatile("s_waitcnt vmcnt(" #n ")" : "+v"(x[0]), "+v"(x[1])); \
        UW(0, h, __builtin_amdgcn_perm(x[1][0], x[0][0], 0x05040100u)); \
        UW(1, h, __builtin_amdgcn_perm(x[1][0], x[0][0], 0x07060302u)); \
        UW(2, h, __builtin_amdgcn_perm(x[1][1], x[0][1], 0x05040100u)); \
        UW(3, h, __builtin_amdgcn_perm(x[1][1], x[0][1], 0x07060302u)); \
        arrive4(sw4, 1u << (12 + 3 * (h))); SB();
        ULOAD(ua);
        ULOAD(uc);
        SB();
        c.uf = opaque(ub + 32 * la + 16 * lg);
        c.sw = opaque(sb + MISC_OFF + 4 * c4);
        c.bd = opaque(sb + B_OFF + 16 * lane);
        const unsigned sw4 = opaque(sb + MISC_OFF + 4 * lane);
        const unsigned ww = opaque(sb + W_OFF + 32 + c4 * W_STRIDE + 32 * la + 8 * lg);
        const u4 B1 = BAND(0);
        asm volatile("s_mov_b32 m0, %0\n s_nop 0" :: "s"(sb + U_OFF + 532 + 256 * c4) : "memory");
        SB();
        ULOAD(ud);
        STAMP(3);
        UPUT(ua, 0, 4);
        asm volatile("" :: "v"(pl), "v"(cpf));
        STAMP(4);
        ULOAD(ua); SB();
        UPUT(uc, 1, 4);
        waitm(c.sw, S_U(0)); wbuild<0>(c, B1, ww); arrive(c.sw, S_W01); SB();
        UPUT(ud, 2, 2);
        STAMP(5);
        waitm(c.sw, S_U(1)); wbuild<2>(c, B1, ww); arrive(c.sw, S_W23); SB();
        UPUT(ua, 3, 0);
        STAMP(6);
        waitm(c.sw, S_U(2)); wbuild<4>(c, B1, ww); arrive(c.sw, S_W45); SB();
        waitm(c.sw, S_U(3)); wbuild<6>(c, B1, ww); arrive(c.sw, S_W6);
        STAMP(7);
        FINISH();
        KILL(cpf); KILL(pl); KILL(pa); KILL(pr); KILL(pi); KILL(ldh); KILL(dkh);
    }
    {
        __builtin_amdgcn_s_setprio(3);
        const unsigned tb = sb + w * T_STRIDE;
        const unsigned tr = opaque(tb + 288 * la + 16 * lg);
        const unsigned pw = opaque(sb + P_OFF + c4 * 6144 + 16 * lane);
        const unsigned pw0 = opaque(pw + (r >> 1) * 1024), pw2 = opaque(pw + (r & 1) * 1024);
        unsigned wk, wpt, st;
        c.sw = opaque(sb + MISC_OFF + 4 * c4);
        if (r == 1) {
            const unsigned u0 = __builtin_amdgcn_perm(0x6D492D48u, 0x6C092C08u, la & 7), u1 = __builtin_amdgcn_perm(0x24406401u, 0x25654100u, la & 7);
            wk = opaque(fgb + 8 * ((la < 8 ? u0 : u1) & 0xffu) + 16 * lg);
            const unsigned wa = opaque((CS + 2) * (lane & 3) + F_REL + 54 - 2 * (la >> 2)), wb = opaque(CS * (lane & 3) + ((lane & 3) == 3 ? 80 : 66 + 2 * (lane & 3)));
            wpt = opaque(fgb + (la < 12 ? wa : wb));
            KILL(st); KILL(c.uf); KILL(c.ut); KILL(c.wt); KILL(c.fa); KILL(c.ga); KILL(c.bd);
        } else {
            COMMON_SETUP();
            st = opaque(tb + 16 * c4 + 8 * la + 128 * lg + 64 * (lg >> 1));
            const unsigned u0 = __builtin_amdgcn_perm(0x80889098u, 0x5D1D5C1Cu, la & 7), u1 = __builtin_amdgcn_perm(0u, 0x81899199u, la & 3);
            const unsigned w2 = opaque(8 * ((la < 8 ? u0 : u1) & 0xffu) + 16 * lg), w0 = opaque(F_REL + 56 + 32 * la + 8 * lg);
            wk = opaque(fgb + (r == 0 ? w0 : w2));
            KILL(wpt);
        }
        const unsigned cnt = opaque(sb + MISC_OFF + 16);
        asm volatile("s_mov_b32 m0, %0\n s_nop 0" :: "s"(tb) : "memory");
        STAMP(3);
        asm volatile("s_waitcnt vmcnt(0)" : "+v"(ldh), "+v"(pl), "+v"(pa), "+v"(pr), "+v"(pi), "+v"(dkh) : "v"(cpf));
        const float LOG2E = 1.4426950408889634f;
        const float dt = __uint_as_float(pkbf(0.f, __builtin_amdgcn_exp2f(__uint_as_float(ldh) * LOG2E)));
        const float arp = __uint_as_float(pkbf(0.f, __builtin_amdgcn_exp2f(__uint_as_float(pl) * LOG2E)));
        const float m1 = arp * (-dt * LOG2E);
        const float th = __builtin_amdgcn_fractf(__uint_as_float(pa) * (dt * 0.15915494309189535f));
        const float mg = __builtin_amdgcn_exp2f(m1);
        f2 z = (f2){__builtin_amdgcn_cosf(th), __builtin_amdgcn_sinf(th)} * mg;
        STAMP(4);
        unsigned mx;
        asm("v_max_f32 %0, |%1|, |%2|\n s_nop 1" : "=v"(mx) : "v"(pr), "v"(pi));
#define MXS(ctl, rm) mx = max(mx, (unsigned)__builtin_amdgcn_update_dpp(0, (int)mx, ctl, rm, 0xf, true))
        const h2 e1 = pkh(z); MXS(0x111, 0xf); z = csq(z); SB();
        const h2 e2 = pkh(z); MXS(0x112, 0xf); z = csq(z); SB();
        const h2 e4 = pkh(z); MXS(0x114, 0xf); z = csq(z); SB();
        const h2 e8 = pkh(z);
        const h2 one = {(_Float16)1.f, (_Float16)0.f};
        h2 t0, t1, t2, t3;
        t0 = cmul(e2, e1); MXS(0x118, 0xf); t1 = cmul(e4, e1); TW(15, one); TW(14, e1); SB();
        const h2 y6 = cmul(e4, e2); MXS(0x142, 0xa); t2 = cmul(e8, e1); TW(13, e2); TW(11, e4); TW(7, e8); SB();
        const h2 y10 = cmul(e8, e2); MXS(0x143, 0xc); const h2 y12 = cmul(e8, e4); TW(12, t0); TW(10, t1); SB();
        const float th2 = th * 16.f;
        const float mg2 = __builtin_amdgcn_exp2f(m1 * 16.f);
        t0 = cmul(y6, e1); const h2 y14 = cmul(e8, y6); TW(9, y6); TW(6, t2); SB();
        z = (f2){__builtin_amdgcn_cosf(th2), __builtin_amdgcn_sinf(th2)} * mg2;
        const unsigned ex = max((unsigned)__builtin_amdgcn_readlane(mx, 63) >> 23, 16u);
        const float sc = __uint_as_float((268u - ex) << 23);
        const float ks = dt * __uint_as_float((ex - 13u) << 23);
        t1 = cmul(y10, e1); t2 = cmul(y12, e1); TW(5, y10); TW(3, y12); SB();
        t3 = cmul(y14, e1);
        TW(8, t0); TW(1, y14); SB();
        const h2 x0 = pkh((f2){__uint_as_float(pr), -__uint_as_float(pi)} * sc);
        const h2 e16 = pkh(z); z = csq(z);
        TW(4, t1); TW(2, t2); TW(0, t3);
        u4 yf[4];
#pragma unroll
        for (int s = 0; s < 4; s++) yf[s] = ldsr<u4>(tr + 64 * s);
        const h2 e32 = pkh(z); z = csq(z);
        SB();
        const h2 x1 = cmulc(x0, e16), x2 = cmulc(x0, e32);
        const h2 e64 = pkh(z); z = csq(z);
        TW(15, x0); SB();
        const h2 x3 = cmulc(x1, e32), x4 = cmulc(x0, e64);
        const h2 e128 = pkh(z); SB();
        const h2 x5 = cmulc(x1, e64), x6 = cmulc(x2, e64); TW(14, x1); TW(13, x2); SB();
        const h2 x7 = cmulc(x3, e64); t0 = cmulc(x0, e128); TW(12, x3); TW(11, x4); SB();
        t1 = cmulc(x1, e128); t2 = cmulc(x2, e128); TW(10, x5); TW(9, x6); SB();
        t3 = cmulc(x3, e128); h2 t4 = cmulc(x4, e128); TW(8, x7); TW(7, t0); SB();
        t0 = cmulc(x5, e128); h2 t5 = cmulc(x6, e128); TW(6, t1); TW(5, t2); SB();
        t1 = cmulc(x7, e128); TW(4, t3); TW(3, t4); SB();
        z = csq(z); TW(2, t0); TW(1, t5); SB();
        TW(0, t1);
        u4 xf[4];
#pragma unroll
        for (int s = 0; s < 4; s++) xf[s] = ldsr<u4>(tr + 64 * s);
        const h2 e256 = pkh(z); z = csq(z);
        const h2 p272 = cmul(e256, e16); t0 = cmul(e256, e4);
        SB();
        t1 = cmul(e256, y6); const h2 p264 = cmul(e256, e8); SB();
        f4 acc0 = zf, acc1 = zf, acc2 = zf;
#pragma unroll
        for (int s = 0; s < 4; s++) acc0 = mfh(yf[s], xf[s], acc0);
        t2 = cmul(e256, y10); t3 = cmul(e256, y12); TW(7, p272); TW(1, t0); SB();
        t4 = cmul(e256, y14); t5 = cmul(p272, e2); TW(2, t1); TW(3, p264); SB();
        const h2 e512 = pkh(z); z = csq(z);
        acc0 *= ks;
        STAMP(5);
        asm volatile("s_cmp_eq_u32 %0, 0\n s_cbranch_scc1 .Lp0_%=\n ds_write_b128 %1, %2\n s_mov_b64 exec, 1\n ds_add_u32 %3, %4\n s_mov_b64 exec, -1\n"
                     ".Lp0_%=:\n s_cmp_eq_u32 %0, 2\n s_cbranch_scc1 .Lp1_%=\n s_setprio 2\n .Lp1_%=:"
                     :: "s"(r), "v"(pw0), "v"(acc0), "v"(c.sw), "v"(1u) : "scc", "memory");
        SB();
        const h2 p520 = cmul(e512, e8), p528 = cmul(e512, e16); TW(4, t2); TW(5, t3); SB();
        const h2 p516 = cmul(e512, e4), p524 = cmul(e512, y12); TW(6, t4); TW(0, t5); SB();
        const h2 e1024 = pkh(z);
        t4 = cmul(e256, e2); TW(12, p520); TW(8, p528); TW(9, e256); SB();
        const h2 p776 = cmul(e256, p520), p784 = cmul(e256, p528); TW(15, p516); TW(11, p524); TW(10, t4);
        SB();
#pragma unroll
        for (int s = 0; s < 4; s++) yf[s] = ldsr<u4>(tr + 64 * s);
        t0 = cmul(e256, p516); t1 = cmul(e256, p524);
        t2 = cmul(e1024, e8); t3 = cmul(e1024, p264); TW(1, p776); TW(3, p784); SB();
#pragma unroll
        for (int s = 0; s < 4; s++) acc1 = mfh(xf[s], yf[s], acc1);
        t4 = cmul(e1024, p520); t5 = cmul(e1024, p776); TW(0, t0); TW(2, t1); SB();
        t0 = cmul(e1024, e16); t1 = cmul(e1024, p272); TW(4, t2); TW(5, t3); SB();
        t2 = cmul(e1024, p528); t3 = cmul(e1024, p784); TW(6, t4); TW(7, t5); SB();
        TW(8, t0); TW(9, t1); TW(10, t2); TW(11, t3);
        SB();
#pragma unroll
        for (int s = 0; s < 4; s++) yf[s] = ldsr<u4>(tr + 64 * s);
        acc1 *= ks;
        SB();
        if (r != 1) { ldsw<f4>(pw0 + 2048, acc1); arrive(c.sw, 4u); }
        KSTAMP(8);
        SB();
#pragma unroll
        for (int s = 0; s < 4; s++) acc2 = mfh(xf[s], yf[s], acc2);
        acc2 *= ks;
        if (r == 2) {
            waitm(c.sw, S_C2);
            f4 kk = acc2 + ldsr<f4>(pw + 4096) + ldsr<f4>(pw + 5120);
            const unsigned q01 = pkbf(kk[0], kk[1]), q23 = pkbf(kk[2], kk[3]);
            if (la < 12) put4g(wk, q01, q23);
            const unsigned k1024 = (__builtin_amdgcn_readlane(q01, 3) & 0xffffu) * 0x10001u;
            arrive(c.sw, S_G2);
            __builtin_amdgcn_s_setprio(1);
            STAMP(6);
            f4 a[4] = {zf, zf, zf, zf};
            u4 p[5], v[5], k[4];
            {
                const u4 B3 = BAND(3), B4 = BAND(4), BF = and4(BAND(5), k1024);
#pragma unroll
                for (int d = 0; d < 4; d++) k[d] = KH(4 + d);
                waits(c.sw, S_U(0) | S_U(1));
                u4 q[4], n[4];
#pragma unroll
                for (int S = 0; S < 4; S++) { p[S] = ufrag(c.uf, S, 18); q[S] = ufrag(c.uf, S, 26); }
#pragma unroll
                for (int S = 0; S < 4; S++) n[S] = ufrag(c.uf, S, 10);
                SB();
#pragma unroll
                for (int S = 0; S < 4; S++) v[S] = bx2(p[S], B3, q[S], B4);
#pragma unroll
                for (int t = 0; t < 4; t++) a[t] = mfb(BF, n[t], a[t]);
            }
            const u4 B2a = BAND(1), B2b = BAND(2);
            SB();
#pragma unroll
            for (int S = 0; S < 4; S++)
#pragma unroll
                for (int t = S; t < 4; t++) a[t] = mfb(v[S], k[t - S], a[t]);
#pragma unroll
            for (int S = 0; S < 4; S++) v[S] = bx2(p[S], B2b, p[S], B2a);
            waits(c.sw, S_G1 | S_W45);
            STAMP(7);
            p[4] = ufrag(c.uf, 4, 18);
            const u4 kd3 = KD(3), kd4 = KD(4);
            u4 vc[6];
#pragma unroll
            for (int S = 0; S < 6; S++) vc[S] = VC(S);
#pragma unroll
            for (int d = 0; d < 4; d++) k[d] = KC(4 - d);
            SB();
            v[4] = bx2(p[4], B2b, p[4], B2a);
#pragma unroll
            for (int t = 0; t < 4; t++) a[t] = mfb(v[t], kd4, a[t]);
#pragma unroll
            for (int t = 0; t < 4; t++) a[t] = mfb(v[t + 1], kd3, a[t]);
            SB();
            u4 k1[4], k2;
#pragma unroll
            for (int S = 0; S < 4; S++) v[S] = VA(2 + S);
            k2 = KA(2); k1[0] = KA(1);
#pragma unroll
            for (int d = 0; d < 4; d++)
#pragma unroll
                for (int t = 0; t < 4; t++) if (t + d < 6) a[t] = mfb(vc[t + d], k[d], a[t]);
            k1[2] = k[3];
            SB();
#pragma unroll
            for (int S = 0; S < 4; S++) p[S] = VB(2 + S);
            k[0] = KB(2); k1[1] = KB(1);
#pragma unroll
            for (int t = 0; t < 4; t++) a[t] = mfb(v[t], k2, a[t]);
#pragma unroll
            for (int t = 0; t < 3; t++) a[t] = mfb(v[t + 1], k1[0], a[t]);
            SB();
#pragma unroll
            for (int S = 0; S < 4; S++) v[S] = VD(2 + S);
            k2 = KD(2); k1[3] = KD(1);
#pragma unroll
            for (int t = 0; t < 4; t++) a[t] = mfb(p[t], k[0], a[t]);
#pragma unroll
            for (int t = 0; t < 3; t++) a[t] = mfb(p[t + 1], k1[1], a[t]);
            SB();
            waitm(c.sw, S_F);
            u4 F[9];
#pragma unroll
            for (int i = 0; i < 9; i++) F[i] = ffrag(c.fa, i);
#pragma unroll
            for (int t = 0; t < 4; t++) a[t] = mfb(v[t], k2, a[t]);
#pragma unroll
            for (int t = 0; t < 3; t++) a[t] = mfb(v[t + 1], k1[3], a[t]);
            STAMP(8);
            near9<4>(c, F, a[0], a[1]);
            STAGE(2, a[0], a[1]);
            STAMP(9);
            waitm(c.sw, S_W6);
            near9<6>(c, F, a[2], a[3]);
            {
                const u4 va = VA(6), vb = VB(6), vc6 = VC(6), vd = VD(6);
                a[3] = mfb(va, k1[0], a[3]); a[3] = mfb(vb, k1[1], a[3]); a[3] = mfb(vc6, k1[2], a[3]); a[3] = mfb(vd, k1[3], a[3]);
            }
            STAGE(3, a[2], a[3]);
            STAMP(10);
            FINISH();
            KILL(acc0); KILL(acc1); KILL(acc2); KILL(wk); KILL(wpt); KILL(dkh);
        }
        ldsw<f4>(pw2 + 4096, acc2);
        arrive(c.sw, 16u);
        __builtin_amdgcn_s_setprio(3);
        KSTAMP(7);
        if (r == 1) {
            waitm(c.sw, S_C1);
            f4 kk = acc1 + ldsr<f4>(pw + 2048) + ldsr<f4>(pw + 3072);
            unsigned q01 = pkbf(kk[0], kk[1]), q23 = pkbf(kk[2], kk[3]);
            const unsigned k256 = __builtin_amdgcn_readlane(q23, 57) >> 16;
            const unsigned k258 = __builtin_amdgcn_readlane(q23, 58) >> 16;
            const unsigned k512 = __builtin_amdgcn_readlane(q01, 7);
            q01 = __builtin_amdgcn_update_dpp(q01, q01, 0xC0, 0xf, 0xc, false);
            q23 = __builtin_amdgcn_update_dpp(q23, q23, 0xC0, 0xf, 0xc, false);
            put4g(wk, q01, q23);
            CFENCE();
            ldsw<unsigned short>(wpt, (unsigned short)(la < 8 ? k256 : (la < 12 ? k258 : k512)));
            arrive(c.sw, S_G1);
            STAMP(6);
            const unsigned ea = opaque(sb + ((lane >> 2) & 3) * (T_STRIDE + 16) + 8 * lg + 128 * (lane & 3) + 32 * (lane & 2) + 64 * (c4 & 1) + 576 * (c4 >> 1));
            auto ro = __builtin_amdgcn_make_buffer_rsrc((void*)ka[7], (short)0, 2048 * 1280, 0x00020000);
            const unsigned vo = opaque((lane + 128 * c4) * 1280 + grp * 8);
#pragma unroll
            for (int pp = 0; pp < 4; pp++) {
                waits(cnt, 4u << (3 * pp));
                const u2 v0 = ldtr(ea + STG(pp)), v1 = ldtr(ea + STG(pp) + 32);
                __builtin_amdgcn_raw_buffer_store_b64(v0, ro, vo, 512 * pp * 1280, 0);
                __builtin_amdgcn_raw_buffer_store_b64(v1, ro, vo, (512 * pp + 64) * 1280, 0);
                STAMP(7 + pp);
                if (pp == WBP && xi == 0 && c4 == 0) asm volatile("s_waitcnt vmcnt(0)\n buffer_wbl2 sc1" ::: "memory");
            }
            FINISH();
            KILL(acc0); KILL(wk); KILL(dkh);
        }
        {
            waitm(c.sw, S_C0);
            f4 kk = acc0 + ldsr<f4>(pw) + ldsr<f4>(pw + 1024);
            kk[3] += lane == 63 ? __uint_as_float(dkh) : 0.f;
            put4(wk, pkbf(kk[0], kk[1]), pkbf(kk[2], kk[3]));
            arrive(c.sw, S_F);
            __builtin_amdgcn_s_setprio(XPRIO);
            STAMP(6);
            f4 a[4] = {zf, zf, zf, zf};
            u4 F[8];
#pragma unroll
            for (int i = 0; i < 8; i++) F[i] = ffrag(c.fa, i);
            waits(c.sw, S_U(0));
            STAMP(7);
            near8<0>(c, F, a[0], a[1]);
            waits(c.sw, S_U(1));
            near8<2>(c, F, a[2], a[3]);
            u4 n[4];
#pragma unroll
            for (int t = 0; t < 4; t++) n[t] = ufrag(c.uf, t, 266);
            const u4 va0 = VA(0), vb0 = VB(0);
            waits(c.sw, S_G1 | S_W01);
            STAMP(8);
            __builtin_amdgcn_s_setprio(2);
            const u4 F8 = ffrag(c.fa, 8), ka1 = KA(1), kb1 = KB(1), vc0 = VC(0), kc1 = KC(1), vd0 = VD(0), kd1 = KD(1);
            SB();
            const u4 va1 = VA(1), vb1 = VB(1), vc1 = VC(1), vd1 = VD(1), ka2 = KA(2), kb2 = KB(2), kc2 = KC(2), kd2 = KD(2);
            a[1] = mfb(va0, ka1, a[1]); a[0] = mfb(F8, n[0], a[0]); a[1] = mfb(vb0, kb1, a[1]); a[1] = mfb(F8, n[1], a[1]);
            a[1] = mfb(vc0, kc1, a[1]); a[1] = mfb(vd0, kd1, a[1]);
            a[2] = mfb(F8, n[2], a[2]); a[3] = mfb(F8, n[3], a[3]);
            STAGE(0, a[0], a[1]);
            STAMP(9);
            const u4 B2a = BAND(1), B2b = BAND(2), p0 = ufrag(c.uf, 0, 18), va2 = VA(2), vb2 = VB(2);
            a[2] = mfb(va1, ka1, a[2]); a[3] = mfb(va1, ka2, a[3]); a[2] = mfb(va0, ka2, a[2]);
            a[2] = mfb(vb1, kb1, a[2]); a[3] = mfb(vb1, kb2, a[3]); a[2] = mfb(vb0, kb2, a[2]);
            waitm(c.sw, S_G2 | S_W23);
            const u4 vc2 = VC(2), vd2 = VD(2), kc3 = KC(3), kd3 = KD(3);
            const u4 w0 = bx2(p0, B2b, p0, B2a);
            a[2] = mfb(vc1, kc1, a[2]); a[3] = mfb(vc1, kc2, a[3]); a[2] = mfb(vc0, kc2, a[2]);
            a[2] = mfb(vd1, kd1, a[2]); a[3] = mfb(vd1, kd2, a[3]); a[2] = mfb(vd0, kd2, a[2]);
            a[3] = mfb(va2, ka1, a[3]); a[3] = mfb(vb2, kb1, a[3]); a[3] = mfb(vc2, kc1, a[3]); a[3] = mfb(vd2, kd1, a[3]);
            a[3] = mfb(vc0, kc3, a[3]); a[3] = mfb(w0, kd3, a[3]);
            STAGE(1, a[2], a[3]);
            STAMP(10);
            FINISH();
        }
    }
}

extern "C" void kernel_launch(void* const* d_in, const int* in_sizes, int n_in, void* d_out, int out_size, void* d_ws, size_t ws_size,
                              hipStream_t stream) {
    s4d_kernel<<<160, 1024, 0, stream>>>((const unsigned short*)d_in[0], (const unsigned short*)d_in[1], (const unsigned short*)d_in[2],
                                         (const unsigned short*)d_in[3], (const unsigned short*)d_in[4], (const unsigned short*)d_in[5],
                                         (const unsigned short*)d_in[6], (unsigned short*)d_out
    );
}
```

```cpp
#define TSPIN 0
#define TROLE -1
#include <hip/hip_runtime.h>
#include <stdint.h>

typedef _Float16 h2 __attribute__((ext_vector_type(2)));
typedef _Float16 h8 __attribute__((ext_vector_type(8)));
typedef __bf16 b2 __attribute__((ext_vector_type(2)));
typedef __bf16 b8 __attribute__((ext_vector_type(8)));
typedef float f2 __attribute__((ext_vector_type(2)));
typedef float f4 __attribute__((ext_vector_type(4)));
typedef unsigned u2 __attribute__((ext_vector_type(2)));
typedef unsigned u4 __attribute__((ext_vector_type(4)));
typedef unsigned long long l8 __attribute__((ext_vector_type(8)));
typedef short s4 __attribute__((ext_vector_type(4)));
#define AS3 __attribute__((address_space(3)))
#define DEV __device__ __forceinline__
#define SB() __builtin_amdgcn_sched_barrier(0)
#define KILL(x) asm volatile("" : "=v"(x))
#define CFENCE() asm volatile("" ::: "memory")

#define NN 192
#define XPRIO 1
#define CODE_BYTES 12288

#define T_STRIDE 4608
#define U_OFF 55296
#define U_STRIDE 4640
#define FG_OFF 73856
#define CS 1984
#define FG_STRIDE (4 * CS)
#define F_REL 1360
#define MISC_OFF (FG_OFF + 4 * FG_STRIDE)
#define W_OFF (MISC_OFF + 64)
#define W_STRIDE 3648
#define P_OFF (W_OFF + 4 * W_STRIDE)
#define B_OFF (P_OFF + 4 * 6144)
#define LDS_BYTES (B_OFF + 6144)
#define S_C0 2u
#define S_C1 8u
#define S_C2 32u
#define S_F 64u
#define S_G1 128u
#define S_G2 256u
#define S_W01 512u
#define S_W23 1024u
#define S_W45 2048u
#define S_U(h) (4u << (12 + 3 * (h)))
#define S_W6 (1u << 24)

#define STAMP(i) do { if ((i) == 0 || (i) == TSPIN) { SB(); ts[i] = __builtin_amdgcn_s_memrealtime(); SB(); } } while (0)

#define KSTAMP(i)
template <class T> DEV T ldsr(unsigned a) { return *(const AS3 T*)(uintptr_t)(a); }
template <class T> DEV T ldsrv(unsigned a) { return *(const volatile AS3 T*)(uintptr_t)(a); }
template <class T> DEV void ldsw(unsigned a, T v) { *(AS3 T*)(uintptr_t)(a) = v; }
DEV u2 ldtr(unsigned a) { s4 v = __builtin_amdgcn_ds_read_tr16_b64_v4i16((AS3 s4*)(uintptr_t)(a)); return __builtin_bit_cast(u2, v); }
DEV int opaques(int x) { asm volatile("" : "+s"(x)); return x; }
DEV unsigned opaque(unsigned x) { asm volatile("" : "+v"(x)); return x; }

DEV unsigned pkbf(float a, float b) { f2 v = {a, b}; return __builtin_bit_cast(unsigned, __builtin_convertvector(v, b2)); }
DEV h2 cmul(h2 h, h2 e) { h2 t = h * (h2){e.x, e.x}; return __builtin_elementwise_fma((h2){-h.y, h.x}, (h2){e.y, e.y}, t); }
DEV h2 cmulc(h2 h, h2 e) { h2 t = h * (h2){e.x, e.x}; return __builtin_elementwise_fma((h2){h.y, -h.x}, (h2){e.y, e.y}, t); }
DEV f4 mfb(u4 a, u4 b, f4 c) { return __builtin_amdgcn_mfma_f32_16x16x32_bf16(__builtin_bit_cast(b8, a), __builtin_bit_cast(b8, b), c, 0, 0, 0); }
DEV f4 mfh(u4 a, u4 b, f4 c) { return __builtin_amdgcn_mfma_f32_16x16x32_f16(__builtin_bit_cast(h8, a), __builtin_bit_cast(h8, b), c, 0, 0, 0); }
DEV u4 cat(u2 a, u2 b) { return (u4){a.x, a.y, b.x, b.y}; }

#define TW(row, val) asm volatile("ds_write_addtid_b32 %0 offset:%1" :: "v"(val), "i"((row) * 288) : "memory")

DEV f2 csq(f2 z) {
    f2 t, r;
    asm("v_pk_mul_f32 %0, %1, %1 op_sel_hi:[0,1]" : "=v"(t) : "v"(z));
    asm("v_pk_fma_f32 %0, %1, %1, %2 op_sel:[1,1,0] op_sel_hi:[0,1,1] neg_lo:[1,0,0]" : "=v"(r) : "v"(z), "v"(t));
    return r;
}
DEV h2 pkh(f2 v) { return __builtin_convertvector(v, h2); }

DEV void arrive(unsigned a, unsigned v) {
    asm volatile("s_mov_b64 exec, 1\n ds_add_u32 %0, %1\n s_mov_b64 exec, -1" :: "v"(a), "v"(v) : "memory");
}
DEV void arrive4(unsigned a, unsigned v) {
    asm volatile("s_mov_b64 exec, 15\n ds_add_u32 %0, %1\n s_mov_b64 exec, -1" :: "v"(a), "v"(v) : "memory");
}
DEV void waitm(unsigned a, unsigned m) {
    CFENCE();
    while ((__builtin_amdgcn_readfirstlane(ldsrv<unsigned>(a)) & m) != m) {}
    CFENCE();
}

DEV void waits(unsigned a, unsigned m) {
    CFENCE();
    while ((__builtin_amdgcn_readfirstlane(ldsrv<unsigned>(a)) & m) != m) __builtin_amdgcn_s_sleep(1);
    CFENCE();
}
DEV h2 oph(h2 x) { asm volatile("" : "+v"(x)); return x; }

DEV void put4(unsigned a, unsigned p01, unsigned p23) {
    unsigned p12 = __builtin_amdgcn_alignbit(p23, p01, 16);
    ldsw<u2>(a, (u2){p01, p23});
    ldsw<unsigned short>(a + CS + 2, (unsigned short)p01);
    ldsw<unsigned>(a + CS + 4, p12);
    ldsw<unsigned short>(a + CS + 8, (unsigned short)(p23 >> 16));
    ldsw<unsigned>(a + 2 * CS + 4, p01);
    ldsw<unsigned>(a + 2 * CS + 8, p23);
    ldsw<unsigned short>(a + 3 * CS + 6, (unsigned short)p01);
    ldsw<unsigned>(a + 3 * CS + 8, p12);
    ldsw<unsigned short>(a + 3 * CS + 12, (unsigned short)(p23 >> 16));
}

DEV void put4g(unsigned a, unsigned p01, unsigned p23) {
    unsigned p12 = __builtin_amdgcn_alignbit(p23, p01, 16);
    ldsw<u2>(a + 3 * CS + 16, (u2){p01, p23});
    ldsw<unsigned short>(a + 2, (unsigned short)p01);
    ldsw<unsigned>(a + 4, p12);
    ldsw<unsigned short>(a + 16, (unsigned short)(p23 >> 16));
    ldsw<unsigned>(a + CS + 4, p01);
    ldsw<unsigned>(a + CS + 16, p23);
    ldsw<unsigned short>(a + 2 * CS + 6, (unsigned short)p01);
    ldsw<unsigned>(a + 2 * CS + 16, p12);
    ldsw<unsigned short>(a + 2 * CS + 20, (unsigned short)(p23 >> 16));
}

struct Ctx {
    unsigned uf;
    unsigned ut;
    unsigned wt;
    unsigned fa, ga;
    unsigned sw;
    unsigned bd;
};

DEV u4 ufrag(unsigned uf, int S, int s0) { return ldsr<u4>(uf + 512 * S + 2 * (266 - s0)); }
DEV u4 gfrag(unsigned ga, int ytop, int d) { return ldsr<u4>(ga + 4 * (ytop + 1 - 16 * d)); }
DEV u4 ffrag(unsigned fa, int i) { return cat(ldsrv<u2>(fa + 544 - 64 * i), ldsrv<u2>(fa + 552 - 64 * i)); }
DEV u2 cv4(f4 d) { return (u2){pkbf(d[0], d[1]), pkbf(d[2], d[3])}; }
DEV u4 utr(const Ctx& c, int S, int p1, int p2) { return cat(ldtr(c.ut + 512 * S + 2 * (266 - p1)), ldtr(c.ut + 512 * S + 2 * (266 - p2))); }
DEV u4 wtr(const Ctx& c, int S, int p1, int p2) { return cat(ldtr(c.wt + 512 * S + 2 * (20 - p1)), ldtr(c.wt + 512 * S + 2 * (20 - p2))); }
#define KA(d) gfrag(c.ga, 47, d)
#define KB(d) gfrag(c.ga, 175, d)
#define KC(d) gfrag(c.ga, 119, d)
#define KD(d) gfrag(c.ga, 247, d)
#define KH(d) gfrag(c.ga, 383, d)
#define VA(S) utr(c, S, 18, 6)
#define VB(S) utr(c, S, 10, 14)
#define VC(S) wtr(c, S, 4, 12)
#define VD(S) wtr(c, S, 8, 16)
#define BAND(i) ldsr<u4>(c.bd + 1024 * (i))
DEV u4 bx2(u4 a, u4 b1, u4 a2, u4 b2) {
    const f4 z = {0.f, 0.f, 0.f, 0.f};
    return cat(cv4(mfb(a, b1, z)), cv4(mfb(a2, b2, z)));
}
DEV u4 and4(u4 m, unsigned v) { return (u4){m[0] & v, m[1] & v, m[2] & v, m[3] & v}; }

template <int R> DEV void near8(const Ctx& c, const u4* F, f4& a0, f4& a1) {
    u4 f0[8], f1[8];
#pragma unroll
    for (int i = 0; i < 3; i++) { f0[i] = ufrag(c.uf, R, 10 + 32 * i); f1[i] = ufrag(c.uf, R + 1, 10 + 32 * i); }
    SB();
#pragma unroll
    for (int i = 3; i < 6; i++) { f0[i] = ufrag(c.uf, R, 10 + 32 * i); f1[i] = ufrag(c.uf, R + 1, 10 + 32 * i); }
#pragma unroll
    for (int i = 0; i < 3; i++) { a0 = mfb(F[i], f0[i], a0); a1 = mfb(F[i], f1[i], a1); }
    SB();
#pragma unroll
    for (int i = 6; i < 8; i++) { f0[i] = ufrag(c.uf, R, 10 + 32 * i); f1[i] = ufrag(c.uf, R + 1, 10 + 32 * i); }
#pragma unroll
    for (int i = 3; i < 6; i++) { a0 = mfb(F[i], f0[i], a0); a1 = mfb(F[i], f1[i], a1); }
    SB();
#pragma unroll
    for (int i = 6; i < 8; i++) { a0 = mfb(F[i], f0[i], a0); a1 = mfb(F[i], f1[i], a1); }
}
template <int R> DEV void near9(const Ctx& c, const u4* F, f4& a0, f4& a1) {
    u4 f0[9], f1[8];
#pragma unroll
    for (int i = 0; i < 3; i++) { f0[i] = ufrag(c.uf, R, 10 + 32 * i); f1[i] = ufrag(c.uf, R + 1, 10 + 32 * i); }
    SB();
#pragma unroll
    for (int i = 3; i < 6; i++) { f0[i] = ufrag(c.uf, R, 10 + 32 * i); f1[i] = ufrag(c.uf, R + 1, 10 + 32 * i); }
#pragma unroll
    for (int i = 0; i < 3; i++) { a0 = mfb(F[i], f0[i], a0); a1 = mfb(F[i], f1[i], a1); }
    a1 = mfb(F[8], f0[0], a1);
    SB();
#pragma unroll
    for (int i = 6; i < 9; i++) { f0[i] = ufrag(c.uf, R, 10 + 32 * i); if (i < 8) f1[i] = ufrag(c.uf, R + 1, 10 + 32 * i); }
#pragma unroll
    for (int i = 3; i < 6; i++) { a0 = mfb(F[i], f0[i], a0); a1 = mfb(F[i], f1[i], a1); }
    SB();
#pragma unroll
    for (int i = 6; i < 9; i++) { a0 = mfb(F[i], f0[i], a0); if (i < 8) a1 = mfb(F[i], f1[i], a1); }
}
template <int S> DEV void wbuild(const Ctx& c, u4 B1, unsigned ww) {
    const f4 z = {0.f, 0.f, 0.f, 0.f};
    const u4 n0 = ufrag(c.uf, S, 10);
    u4 n1;
    if (S < 6) n1 = ufrag(c.uf, S + 1, 10);
    ldsw<u2>(ww + 512 * S, cv4(mfb(B1, n0, z)));
    if (S < 6) ldsw<u2>(ww + 512 * (S + 1), cv4(mfb(B1, n1, z)));
}

#define PROF_OUT() \
    asm volatile("s_waitcnt vmcnt(0)" ::: "memory"); \
    STAMP(12); \
    { unsigned long long tgt = ts[0] + 3000 + (TROLE < 0 || r == TROLE ? 4 * (ts[TSPIN] - ts[0]) : 0); \
      if ((bx & 7) == 0) while (__builtin_amdgcn_s_memrealtime() < tgt) __builtin_amdgcn_s_sleep(2); }
#define STG(pp) (((pp) >> 1) * 8 * T_STRIDE + ((pp) & 1) * 2048)
#define STAGE(pp, x0, x1) do { ldsw<u2>(st + ((pp) & 1) * 2048, cv4(x0)); ldsw<u2>(st + ((pp) & 1) * 2048 + 576, cv4(x1)); \
    arrive(cnt, 1u << (3 * (pp))); } while (0)
#define FINISH() do { PROF_OUT(); asm volatile("s_endpgm" ::: "memory"); } while (0)

__global__ __launch_bounds__(1024, 4) void s4d_kernel(const unsigned short* __restrict__ u, const unsigned short* __restrict__ lar,
                                                      const unsigned short* __restrict__ aim, const unsigned short* __restrict__ cre,
                                                      const unsigned short* __restrict__ cim, const unsigned short* __restrict__ ldt,
                                                      const unsigned short* __restrict__ dsk, unsigned short* __restrict__ out
) {
    __shared__ __attribute__((aligned(16))) char smem[LDS_BYTES];
    const unsigned sb = (unsigned)(uintptr_t)(AS3 char*)smem;
    unsigned long long ts[16];
    for (int i = 0; i < 16; i++) ts[i] = 0;
    STAMP(0);
    const int tid = threadIdx.x;
    l8 ka;
    unsigned cpf;
    {
        unsigned long long pc;
        asm volatile("s_load_dwordx16 %0, %2, 0x0\n"
                     "s_getpc_b64 %1" : "=&s"(ka), "=&s"(pc) : "s"(__builtin_amdgcn_kernarg_segment_ptr()));
        asm volatile("v_lshlrev_b32 %0, 7, %2\n"
                     "v_min_u32 %0, %3, %0\n"
                     "global_load_dword %0, %0, %1"
                     : "=&v"(cpf) : "s"(pc), "v"(tid), "i"(CODE_BYTES));
    }
    SB();
    const int lane = tid & 63;
    const int w = __builtin_amdgcn_readfirstlane(tid >> 6);
    const int c4 = w & 3, r = w >> 2;
    const int bx = blockIdx.x;
    const int xi = bx >> 3;
    const int grp = (bx & 7) * 20 + xi;
    const int ch = grp * 4 + c4;
    const int la = lane & 15, lg = lane >> 4;

    const unsigned ub = sb + U_OFF + c4 * U_STRIDE;
    const unsigned fgb = sb + FG_OFF + c4 * FG_STRIDE;
    const f4 zf = {0.f, 0.f, 0.f, 0.f};
    Ctx c;
#define COMMON_SETUP() \
    c.uf = opaque(ub + 32 * la + 16 * lg); \
    c.ut = opaque(ub + 8 * lane); \
    c.wt = opaque(sb + W_OFF + c4 * W_STRIDE + 8 * lane); \
    c.fa = opaque(fgb + F_REL + (CS + 2) * ((la + 3) & 3) - 2 * la + 16 * lg + 2); \
    c.ga = opaque(fgb + CS * (la & 3) - 4 * (la & 12) + 16 * lg); \
    c.bd = opaque(sb + B_OFF + 16 * lane);
    unsigned pl, pa, pr, pi, ldh, dkh, uo;
    const u4 z4 = {0u, 0u, 0u, 0u};
    const unsigned za = opaque(sb + FG_OFF + tid * 16);
    if (r == 3) {
        const int lt = tid & 255;
        ldsw<u4>(za + 12288, z4);
        ldsw<u4>(za + 16384 - 960, z4);
        const unsigned zj = min((unsigned)lt >> 2, 39u);
        const unsigned z1 = opaque(U_OFF + zj * 16), z2 = opaque(U_OFF + zj * 1024 + (528 - 33 * 1024)), z3 = opaque(W_OFF + (zj - 38) * 16);
        ldsw<u4>(sb + (lt & 3) * (zj < 38 ? U_STRIDE : W_STRIDE) + (zj < 34 ? z1 : (zj < 38 ? z2 : z3)), z4);
        SB();
        const unsigned idx = min(lt * 20 + xi, 4095);
        const unsigned poff = opaque((idx >> 1) * 1280 + (bx & 7) * 160 + (idx & 1) * 156);
        uo = opaque(lt * 2560 + grp * 8);
        KILL(pa); KILL(pr); KILL(pi); KILL(ldh); KILL(dkh);
        asm volatile("s_waitcnt lgkmcnt(0)" :: "s"(ka) : "memory");
        asm volatile("global_load_dword %0, %1, %2" : "=v"(pl) : "v"(poff), "s"(ka[0]));
    } else {
        ldsw<u4>(za, z4); ldsw<u4>(za + 12288, z4);
        const unsigned vc = opaque(2 * ch), vo = opaque(2 * (ch * NN + r * 64 + lane));
        if (w < 6) {
            const unsigned pat = (unsigned)((w < 4 ? 0x3F801F00001F00E0ull : 0xC07FC0ull) >> (16 * c4)) & 0xffffu;
            const unsigned val = w == 5 ? 0xffffu : 0x3F80u;
            const unsigned by = (pat << la >> (8 * lg)) & 0xffu;
            const unsigned e = by | (by << 15);
            u4 bq;
#pragma unroll
            for (int m = 0; m < 4; m++) bq[m] = __umul24((e >> (2 * m)) & 0x10001u, val);
            ldsw<u4>(za + (B_OFF - FG_OFF), bq);
        }
        ldh = opaque(0u); pl = opaque(0u); pa = opaque(0u); pr = opaque(0u); pi = opaque(0u); dkh = opaque(0u);
        SB();
        asm volatile("s_waitcnt lgkmcnt(0)" :: "s"(ka) : "memory");
        asm volatile("global_load_short_d16_hi %0, %1, %2" : "+v"(ldh) : "v"(vc), "s"(ka[5]));
        asm volatile("global_load_short_d16_hi %0, %1, %2" : "+v"(pl) : "v"(vo), "s"(ka[1]));
        asm volatile("global_load_short_d16_hi %0, %1, %2" : "+v"(pa) : "v"(vo), "s"(ka[2]));
        asm volatile("global_load_short_d16_hi %0, %1, %2" : "+v"(pr) : "v"(vo), "s"(ka[3]));
        asm volatile("global_load_short_d16_hi %0, %1, %2" : "+v"(pi) : "v"(vo), "s"(ka[4]));
        asm volatile("global_load_short_d16_hi %0, %1, %2" : "+v"(dkh) : "v"(vc), "s"(ka[6]));
        KILL(uo);
    }
    STAMP(1);
    asm volatile("s_barrier" ::: "memory");
    STAMP(2);
    SB();
    if (opaques(r) == 3) {
        __builtin_amdgcn_s_setprio(3);
        u2 ua[2], uc[2], ud[2];
#define ULOAD(x) \
        asm volatile("global_load_dwordx2 %0, %1, %2" : "=v"(x[0]) : "v"(uo), "s"(ka[0])); \
        asm volatile("global_load_dwordx2 %0, %1, %2 offset:1280" : "=v"(x[1]) : "v"(uo), "s"(ka[0])); \
        uo += 512 * 1280;
#define UW(cc, h, val) asm volatile("ds_write_addtid_b32 %0 offset:%1" :: "v"(val), "i"((cc) * U_STRIDE + 1024 * (h)) : "memory")
#define UPUT(x, h, n) \
        asm volatile("s_waitcnt vmcnt(" #n ")" : "+v"(x[0]), "+v"(x[1])); \
        UW(0, h, __builtin_amdgcn_perm(x[1][0], x[0][0], 0x05040100u)); \
        UW(1, h, __builtin_amdgcn_perm(x[1][0], x[0][0], 0x07060302u)); \
        UW(2, h, __builtin_amdgcn_perm(x[1][1], x[0][1], 0x05040100u)); \
        UW(3, h, __builtin_amdgcn_perm(x[1][1], x[0][1], 0x07060302u)); \
        arrive4(sw4, 1u << (12 + 3 * (h))); SB();
        ULOAD(ua);
        ULOAD(uc);
        SB();
        c.uf = opaque(ub + 32 * la + 16 * lg);
        c.sw = opaque(sb + MISC_OFF + 4 * c4);
        c.bd = opaque(sb + B_OFF + 16 * lane);
        const unsigned sw4 = opaque(sb + MISC_OFF + 4 * lane);
        const unsigned ww = opaque(sb + W_OFF + 32 + c4 * W_STRIDE + 32 * la + 8 * lg);
        const u4 B1 = BAND(0);
        asm volatile("s_mov_b32 m0, %0\n s_nop 0" :: "s"(sb + U_OFF + 532 + 256 * c4) : "memory");
        SB();
        ULOAD(ud);
        STAMP(3);
        UPUT(ua, 0, 4);
        asm volatile("" :: "v"(pl), "v"(cpf));
        STAMP(4);
        ULOAD(ua); SB();
        UPUT(uc, 1, 4);
        waitm(c.sw, S_U(0)); wbuild<0>(c, B1, ww); arrive(c.sw, S_W01); SB();
        UPUT(ud, 2, 2);
        STAMP(5);
        waitm(c.sw, S_U(1)); wbuild<2>(c, B1, ww); arrive(c.sw, S_W23); SB();
        UPUT(ua, 3, 0);
        STAMP(6);
        waitm(c.sw, S_U(2)); wbuild<4>(c, B1, ww); arrive(c.sw, S_W45); SB();
        waitm(c.sw, S_U(3)); wbuild<6>(c, B1, ww); arrive(c.sw, S_W6);
        STAMP(7);
        FINISH();
        KILL(cpf); KILL(pl); KILL(pa); KILL(pr); KILL(pi); KILL(ldh); KILL(dkh);
    }
    {
        __builtin_amdgcn_s_setprio(3);
        const unsigned tb = sb + w * T_STRIDE;
        const unsigned tr = opaque(tb + 288 * la + 16 * lg);
        const unsigned pw = opaque(sb + P_OFF + c4 * 6144 + 16 * lane);
        const unsigned pw0 = opaque(pw + (r >> 1) * 1024), pw2 = opaque(pw + (r & 1) * 1024);
        unsigned wk, wpt, st;
        c.sw = opaque(sb + MISC_OFF + 4 * c4);
        if (r == 1) {
            const unsigned u0 = __builtin_amdgcn_perm(0x6D492D48u, 0x6C092C08u, la & 7), u1 = __builtin_amdgcn_perm(0x24406401u, 0x25654100u, la & 7);
            wk = opaque(fgb + 8 * ((la < 8 ? u0 : u1) & 0xffu) + 16 * lg);
            const unsigned wa = opaque((CS + 2) * (lane & 3) + F_REL + 54 - 2 * (la >> 2)), wb = opaque(CS * (lane & 3) + ((lane & 3) == 3 ? 80 : 66 + 2 * (lane & 3)));
            wpt = opaque(fgb + (la < 12 ? wa : wb));
            KILL(st); KILL(c.uf); KILL(c.ut); KILL(c.wt); KILL(c.fa); KILL(c.ga); KILL(c.bd);
        } else {
            COMMON_SETUP();
            st = opaque(tb + 16 * c4 + 8 * la + 128 * lg + 64 * (lg >> 1));
            const unsigned u0 = __builtin_amdgcn_perm(0x80889098u, 0x5D1D5C1Cu, la & 7), u1 = __builtin_amdgcn_perm(0u, 0x81899199u, la & 3);
            const unsigned w2 = opaque(8 * ((la < 8 ? u0 : u1) & 0xffu) + 16 * lg), w0 = opaque(F_REL + 56 + 32 * la + 8 * lg);
            wk = opaque(fgb + (r == 0 ? w0 : w2));
            KILL(wpt);
        }
        const unsigned cnt = opaque(sb + MISC_OFF + 16);
        asm volatile("s_mov_b32 m0, %0\n s_nop 0" :: "s"(tb) : "memory");
        STAMP(3);
        asm volatile("s_waitcnt vmcnt(0)" : "+v"(ldh), "+v"(pl), "+v"(pa), "+v"(pr), "+v"(pi), "+v"(dkh) : "v"(cpf));
        const float LOG2E = 1.4426950408889634f;
        const float dt = __uint_as_float(pkbf(0.f, __builtin_amdgcn_exp2f(__uint_as_float(ldh) * LOG2E)));
        const float arp = __uint_as_float(pkbf(0.f, __builtin_amdgcn_exp2f(__uint_as_float(pl) * LOG2E)));
        const float m1 = arp * (-dt * LOG2E);
        const float th = __builtin_amdgcn_fractf(__uint_as_float(pa) * (dt * 0.15915494309189535f));
        const float mg = __builtin_amdgcn_exp2f(m1);
        f2 z = (f2){__builtin_amdgcn_cosf(th), __builtin_amdgcn_sinf(th)} * mg;
        STAMP(4);
        unsigned mx;
        asm("v_max_f32 %0, |%1|, |%2|\n s_nop 1" : "=v"(mx) : "v"(pr), "v"(pi));
#define MXS(ctl, rm) mx = max(mx, (unsigned)__builtin_amdgcn_update_dpp(0, (int)mx, ctl, rm, 0xf, true))
        const h2 e1 = pkh(z); MXS(0x111, 0xf); z = csq(z); SB();
        const h2 e2 = pkh(z); MXS(0x112, 0xf); z = csq(z); SB();
        const h2 e4 = pkh(z); MXS(0x114, 0xf); z = csq(z); SB();
        const h2 e8 = pkh(z);
        const h2 one = {(_Float16)1.f, (_Float16)0.f};
        h2 t0, t1, t2, t3;
        t0 = cmul(e2, e1); MXS(0x118, 0xf); t1 = cmul(e4, e1); TW(15, one); TW(14, e1); SB();
        const h2 y6 = cmul(e4, e2); MXS(0x142, 0xa); t2 = cmul(e8, e1); TW(13, e2); TW(11, e4); TW(7, e8); SB();
        const h2 y10 = cmul(e8, e2); MXS(0x143, 0xc); const h2 y12 = cmul(e8, e4); TW(12, t0); TW(10, t1); SB();
        const float th2 = th * 16.f;
        const float mg2 = __builtin_amdgcn_exp2f(m1 * 16.f);
        t0 = cmul(y6, e1); const h2 y14 = cmul(e8, y6); TW(9, y6); TW(6, t2); SB();
        z = (f2){__builtin_amdgcn_cosf(th2), __builtin_amdgcn_sinf(th2)} * mg2;
        const unsigned ex = max((unsigned)__builtin_amdgcn_readlane(mx, 63) >> 23, 16u);
        const float sc = __uint_as_float((268u - ex) << 23);
        const float ks = dt * __uint_as_float((ex - 13u) << 23);
        t1 = cmul(y10, e1); t2 = cmul(y12, e1); TW(5, y10); TW(3, y12); SB();
        t3 = cmul(y14, e1);
        TW(8, t0); TW(1, y14); SB();
        const h2 x0 = pkh((f2){__uint_as_float(pr), -__uint_as_float(pi)} * sc);
        const h2 e16 = pkh(z); z = csq(z);
        TW(4, t1); TW(2, t2); TW(0, t3);
        u4 yf[4];
#pragma unroll
        for (int s = 0; s < 4; s++) yf[s] = ldsr<u4>(tr + 64 * s);
        const h2 e32 = pkh(z); z = csq(z);
        SB();
        const h2 x1 = cmulc(x0, e16), x2 = cmulc(x0, e32);
        const h2 e64 = pkh(z); z = csq(z);
        TW(15, x0); SB();
        const h2 x3 = cmulc(x1, e32), x4 = cmulc(x0, e64);
        const h2 e128 = pkh(z); SB();
        const h2 x5 = cmulc(x1, e64), x6 = cmulc(x2, e64); TW(14, x1); TW(13, x2); SB();
        const h2 x7 = cmulc(x3, e64); t0 = cmulc(x0, e128); TW(12, x3); TW(11, x4); SB();
        t1 = cmulc(x1, e128); t2 = cmulc(x2, e128); TW(10, x5); TW(9, x6); SB();
        t3 = cmulc(x3, e128); h2 t4 = cmulc(x4, e128); TW(8, x7); TW(7, t0); SB();
        t0 = cmulc(x5, e128); h2 t5 = cmulc(x6, e128); TW(6, t1); TW(5, t2); SB();
        t1 = cmulc(x7, e128); TW(4, t3); TW(3, t4); SB();
        z = csq(z); TW(2, t0); TW(1, t5); SB();
        TW(0, t1);
        u4 xf[4];
#pragma unroll
        for (int s = 0; s < 4; s++) xf[s] = ldsr<u4>(tr + 64 * s);
        const h2 e256 = pkh(z); z = csq(z);
        const h2 p272 = cmul(e256, e16); t0 = cmul(e256, e4);
        SB();
        t1 = cmul(e256, y6); const h2 p264 = cmul(e256, e8); SB();
        f4 acc0 = zf, acc1 = zf, acc2 = zf;
#pragma unroll
        for (int s = 0; s < 4; s++) acc0 = mfh(yf[s], xf[s], acc0);
        t2 = cmul(e256, y10); t3 = cmul(e256, y12); TW(7, p272); TW(1, t0); SB();
        t4 = cmul(e256, y14); t5 = cmul(p272, e2); TW(2, t1); TW(3, p264); SB();
        const h2 e512 = pkh(z); z = csq(z);
        acc0 *= ks;
        STAMP(5);
        asm volatile("s_cmp_eq_u32 %0, 0\n s_cbranch_scc1 .Lp0_%=\n ds_write_b128 %1, %2\n s_mov_b64 exec, 1\n ds_add_u32 %3, %4\n s_mov_b64 exec, -1\n"
                     ".Lp0_%=:\n s_cmp_eq_u32 %0, 2\n s_cbranch_scc1 .Lp1_%=\n s_setprio 2\n .Lp1_%=:"
                     :: "s"(r), "v"(pw0), "v"(acc0), "v"(c.sw), "v"(1u) : "scc", "memory");
        SB();
        const h2 p520 = cmul(e512, e8), p528 = cmul(e512, e16); TW(4, t2); TW(5, t3); SB();
        const h2 p516 = cmul(e512, e4), p524 = cmul(e512, y12); TW(6, t4); TW(0, t5); SB();
        const h2 e1024 = pkh(z);
        t4 = cmul(e256, e2); TW(12, p520); TW(8, p528); TW(9, e256); SB();
        const h2 p776 = cmul(e256, p520), p784 = cmul(e256, p528); TW(15, p516); TW(11, p524); TW(10, t4);
        SB();
#pragma unroll
        for (int s = 0; s < 4; s++) yf[s] = ldsr<u4>(tr + 64 * s);
        t0 = cmul(e256, p516); t1 = cmul(e256, p524);
        t2 = cmul(e1024, e8); t3 = cmul(e1024, p264); TW(1, p776); TW(3, p784); SB();
#pragma unroll
        for (int s = 0; s < 4; s++) acc1 = mfh(xf[s], yf[s], acc1);
        t4 = cmul(e1024, p520); t5 = cmul(e1024, p776); TW(0, t0); TW(2, t1); SB();
        t0 = cmul(e1024, e16); t1 = cmul(e1024, p272); TW(4, t2); TW(5, t3); SB();
        t2 = cmul(e1024, p528); t3 = cmul(e1024, p784); TW(6, t4); TW(7, t5); SB();
        TW(8, t0); TW(9, t1); TW(10, t2); TW(11, t3);
        SB();
#pragma unroll
        for (int s = 0; s < 4; s++) yf[s] = ldsr<u4>(tr + 64 * s);
        acc1 *= ks;
        SB();
        if (r != 1) { ldsw<f4>(pw0 + 2048, acc1); arrive(c.sw, 4u); }
        KSTAMP(8);
        SB();
#pragma unroll
        for (int s = 0; s < 4; s++) acc2 = mfh(xf[s], yf[s], acc2);
        acc2 *= ks;
        if (r == 2) {
            waitm(c.sw, S_C2);
            f4 kk = acc2 + ldsr<f4>(pw + 4096) + ldsr<f4>(pw + 5120);
            const unsigned q01 = pkbf(kk[0], kk[1]), q23 = pkbf(kk[2], kk[3]);
            if (la < 12) put4g(wk, q01, q23);
            const unsigned k1024 = (__builtin_amdgcn_readlane(q01, 3) & 0xffffu) * 0x10001u;
            arrive(c.sw, S_G2);
            __builtin_amdgcn_s_setprio(1);
            STAMP(6);
            f4 a[4] = {zf, zf, zf, zf};
            u4 p[5], v[5], k[4];
            {
                const u4 B3 = BAND(3), B4 = BAND(4), BF = and4(BAND(5), k1024);
#pragma unroll
                for (int d = 0; d < 4; d++) k[d] = KH(4 + d);
                waits(c.sw, S_U(0) | S_U(1));
                u4 q[4], n[4];
#pragma unroll
                for (int S = 0; S < 4; S++) { p[S] = ufrag(c.uf, S, 18); q[S] = ufrag(c.uf, S, 26); }
#pragma unroll
                for (int S = 0; S < 4; S++) n[S] = ufrag(c.uf, S, 10);
                SB();
#pragma unroll
                for (int S = 0; S < 4; S++) v[S] = bx2(p[S], B3, q[S], B4);
#pragma unroll
                for (int t = 0; t < 4; t++) a[t] = mfb(BF, n[t], a[t]);
            }
            const u4 B2a = BAND(1), B2b = BAND(2);
            SB();
#pragma unroll
            for (int S = 0; S < 4; S++)
#pragma unroll
                for (int t = S; t < 4; t++) a[t] = mfb(v[S], k[t - S], a[t]);
#pragma unroll
            for (int S = 0; S < 4; S++) v[S] = bx2(p[S], B2b, p[S], B2a);
            waits(c.sw, S_G1 | S_W45);
            STAMP(7);
            p[4] = ufrag(c.uf, 4, 18);
            const u4 kd3 = KD(3), kd4 = KD(4);
            u4 vc[6];
#pragma unroll
            for (int S = 0; S < 6; S++) vc[S] = VC(S);
#pragma unroll
            for (int d = 0; d < 4; d++) k[d] = KC(4 - d);
            SB();
            v[4] = bx2(p[4], B2b, p[4], B2a);
#pragma unroll
            for (int t = 0; t < 4; t++) a[t] = mfb(v[t], kd4, a[t]);
#pragma unroll
            for (int t = 0; t < 4; t++) a[t] = mfb(v[t + 1], kd3, a[t]);
            SB();
            u4 k1[4], k2;
#pragma unroll
            for (int S = 0; S < 4; S++) v[S] = VA(2 + S);
            k2 = KA(2); k1[0] = KA(1);
#pragma unroll
            for (int d = 0; d < 4; d++)
#pragma unroll
                for (int t = 0; t < 4; t++) if (t + d < 6) a[t] = mfb(vc[t + d], k[d], a[t]);
            k1[2] = k[3];
            SB();
#pragma unroll
            for (int S = 0; S < 4; S++) p[S] = VB(2 + S);
            k[0] = KB(2); k1[1] = KB(1);
#pragma unroll
            for (int t = 0; t < 4; t++) a[t] = mfb(v[t], k2, a[t]);
#pragma unroll
            for (int t = 0; t < 3; t++) a[t] = mfb(v[t + 1], k1[0], a[t]);
            SB();
#pragma unroll
            for (int S = 0; S < 4; S++) v[S] = VD(2 + S);
            k2 = KD(2); k1[3] = KD(1);
#pragma unroll
            for (int t = 0; t < 4; t++) a[t] = mfb(p[t], k[0], a[t]);
#pragma unroll
            for (int t = 0; t < 3; t++) a[t] = mfb(p[t + 1], k1[1], a[t]);
            SB();
            waitm(c.sw, S_F);
            u4 F[9];
#pragma unroll
            for (int i = 0; i < 9; i++) F[i] = ffrag(c.fa, i);
#pragma unroll
            for (int t = 0; t < 4; t++) a[t] = mfb(v[t], k2, a[t]);
#pragma unroll
            for (int t = 0; t < 3; t++) a[t] = mfb(v[t + 1], k1[3], a[t]);
            STAMP(8);
            near9<4>(c, F, a[0], a[1]);
            STAGE(2, a[0], a[1]);
            STAMP(9);
            waitm(c.sw, S_W6);
            near9<6>(c, F, a[2], a[3]);
            {
                const u4 va = VA(6), vb = VB(6), vc6 = VC(6), vd = VD(6);
                a[3] = mfb(va, k1[0], a[3]); a[3] = mfb(vb, k1[1], a[3]); a[3] = mfb(vc6, k1[2], a[3]); a[3] = mfb(vd, k1[3], a[3]);
            }
            STAGE(3, a[2], a[3]);
            STAMP(10);
            FINISH();
            KILL(acc0); KILL(acc1); KILL(acc2); KILL(wk); KILL(wpt); KILL(dkh);
        }
        ldsw<f4>(pw2 + 4096, acc2);
        arrive(c.sw, 16u);
        __builtin_amdgcn_s_setprio(3);
        KSTAMP(7);
        if (r == 1) {
            waitm(c.sw, S_C1);
            f4 kk = acc1 + ldsr<f4>(pw + 2048) + ldsr<f4>(pw + 3072);
            unsigned q01 = pkbf(kk[0], kk[1]), q23 = pkbf(kk[2], kk[3]);
            const unsigned k256 = __builtin_amdgcn_readlane(q23, 57) >> 16;
            const unsigned k258 = __builtin_amdgcn_readlane(q23, 58) >> 16;
            const unsigned k512 = __builtin_amdgcn_readlane(q01, 7);
            q01 = __builtin_amdgcn_update_dpp(q01, q01, 0xC0, 0xf, 0xc, false);
            q23 = __builtin_amdgcn_update_dpp(q23, q23, 0xC0, 0xf, 0xc, false);
            put4g(wk, q01, q23);
            CFENCE();
            ldsw<unsigned short>(wpt, (unsigned short)(la < 8 ? k256 : (la < 12 ? k258 : k512)));
            arrive(c.sw, S_G1);
            STAMP(6);
            const unsigned ea = opaque(sb + ((lane >> 2) & 3) * (T_STRIDE + 16) + 8 * lg + 128 * (lane & 3) + 32 * (lane & 2) + 64 * (c4 & 1) + 576 * (c4 >> 1));
            auto ro = __builtin_amdgcn_make_buffer_rsrc((void*)ka[7], (short)0, 2048 * 1280, 0x00020000);
            const unsigned vo = opaque((lane + 128 * c4) * 1280 + grp * 8);
#pragma unroll
            for (int pp = 0; pp < 4; pp++) {
                waits(cnt, 4u << (3 * pp));
                const u2 v0 = ldtr(ea + STG(pp)), v1 = ldtr(ea + STG(pp) + 32);
                __builtin_amdgcn_raw_buffer_store_b64(v0, ro, vo, 512 * pp * 1280, 0);
                __builtin_amdgcn_raw_buffer_store_b64(v1, ro, vo, (512 * pp + 64) * 1280, 0);
                STAMP(7 + pp);
            }
            FINISH();
            KILL(acc0); KILL(wk); KILL(dkh);
        }
        {
            waitm(c.sw, S_C0);
            f4 kk = acc0 + ldsr<f4>(pw) + ldsr<f4>(pw + 1024);
            kk[3] += lane == 63 ? __uint_as_float(dkh) : 0.f;
            put4(wk, pkbf(kk[0], kk[1]), pkbf(kk[2], kk[3]));
            arrive(c.sw, S_F);
            __builtin_amdgcn_s_setprio(XPRIO);
            STAMP(6);
            f4 a[4] = {zf, zf, zf, zf};
            u4 F[8];
#pragma unroll
            for (int i = 0; i < 8; i++) F[i] = ffrag(c.fa, i);
            waits(c.sw, S_U(0));
            STAMP(7);
            near8<0>(c, F, a[0], a[1]);
            waits(c.sw, S_U(1));
            near8<2>(c, F, a[2], a[3]);
            u4 n[4];
#pragma unroll
            for (int t = 0; t < 4; t++) n[t] = ufrag(c.uf, t, 266);
            const u4 va0 = VA(0), vb0 = VB(0);
            waits(c.sw, S_G1 | S_W01);
            STAMP(8);
            __builtin_amdgcn_s_setprio(2);
            const u4 F8 = ffrag(c.fa, 8), ka1 = KA(1), kb1 = KB(1), vc0 = VC(0), kc1 = KC(1), vd0 = VD(0), kd1 = KD(1);
            SB();
            const u4 va1 = VA(1), vb1 = VB(1), vc1 = VC(1), vd1 = VD(1), ka2 = KA(2), kb2 = KB(2), kc2 = KC(2), kd2 = KD(2);
            a[1] = mfb(va0, ka1, a[1]); a[0] = mfb(F8, n[0], a[0]); a[1] = mfb(vb0, kb1, a[1]); a[1] = mfb(F8, n[1], a[1]);
            a[1] = mfb(vc0, kc1, a[1]); a[1] = mfb(vd0, kd1, a[1]);
            a[2] = mfb(F8, n[2], a[2]); a[3] = mfb(F8, n[3], a[3]);
            STAGE(0, a[0], a[1]);
            STAMP(9);
            const u4 B2a = BAND(1), B2b = BAND(2), p0 = ufrag(c.uf, 0, 18), va2 = VA(2), vb2 = VB(2);
            a[2] = mfb(va1, ka1, a[2]); a[3] = mfb(va1, ka2, a[3]); a[2] = mfb(va0, ka2, a[2]);
            a[2] = mfb(vb1, kb1, a[2]); a[3] = mfb(vb1, kb2, a[3]); a[2] = mfb(vb0, kb2, a[2]);
            waitm(c.sw, S_G2 | S_W23);
            const u4 vc2 = VC(2), vd2 = VD(2), kc3 = KC(3), kd3 = KD(3);
            const u4 w0 = bx2(p0, B2b, p0, B2a);
            a[2] = mfb(vc1, kc1, a[2]); a[3] = mfb(vc1, kc2, a[3]); a[2] = mfb(vc0, kc2, a[2]);
            a[2] = mfb(vd1, kd1, a[2]); a[3] = mfb(vd1, kd2, a[3]); a[2] = mfb(vd0, kd2, a[2]);
            a[3] = mfb(va2, ka1, a[3]); a[3] = mfb(vb2, kb1, a[3]); a[3] = mfb(vc2, kc1, a[3]); a[3] = mfb(vd2, kd1, a[3]);
            a[3] = mfb(vc0, kc3, a[3]); a[3] = mfb(w0, kd3, a[3]);
            STAGE(1, a[2], a[3]);
            STAMP(10);
            FINISH();
        }
    }
}

extern "C" void kernel_launch(void* const* d_in, const int* in_sizes, int n_in, void* d_out, int out_size, void* d_ws, size_t ws_size,
                              hipStream_t stream) {
    s4d_kernel<<<160, 1024, 0, stream>>>((const unsigned short*)d_in[0], (const unsigned short*)d_in[1], (const unsigned short*)d_in[2],
                                         (const unsigned short*)d_in[3], (const unsigned short*)d_in[4], (const unsigned short*)d_in[5],
                                         (const unsigned short*)d_in[6], (unsigned short*)d_out
    );
}
```

```cpp
#include <hip/hip_runtime.h>
#include <stdint.h>

typedef _Float16 h2 __attribute__((ext_vector_type(2)));
typedef _Float16 h8 __attribute__((ext_vector_type(8)));
typedef __bf16 b2 __attribute__((ext_vector_type(2)));
typedef __bf16 b8 __attribute__((ext_vector_type(8)));
typedef float f2 __attribute__((ext_vector_type(2)));
typedef float f4 __attribute__((ext_vector_type(4)));
typedef unsigned u2 __attribute__((ext_vector_type(2)));
typedef unsigned u4 __attribute__((ext_vector_type(4)));
typedef unsigned long long l8 __attribute__((ext_vector_type(8)));
typedef short s4 __attribute__((ext_vector_type(4)));
#define AS3 __attribute__((address_space(3)))
#define DEV __device__ __forceinline__
#define SB() __builtin_amdgcn_sched_barrier(0)
#define KILL(x) asm volatile("" : "=v"(x))
#define CFENCE() asm volatile("" ::: "memory")

#define NN 192
#define XPRIO 1
#define CODE_BYTES 12288

#define T_STRIDE 4608
#define U_OFF 55296
#define U_STRIDE 4640
#define FG_OFF 73856
#define CS 1984
#define FG_STRIDE (4 * CS)
#define F_REL 1360
#define MISC_OFF (FG_OFF + 4 * FG_STRIDE)
#define W_OFF (MISC_OFF + 64)
#define W_STRIDE 3648
#define P_OFF (W_OFF + 4 * W_STRIDE)
#define B_OFF (P_OFF + 4 * 6144)
#define LDS_BYTES (B_OFF + 6144)
#define S_C0 2u
#define S_C1 8u
#define S_C2 32u
#define S_F 64u
#define S_G1 128u
#define S_G2 256u
#define S_W01 512u
#define S_W23 1024u
#define S_W45 2048u
#define S_U(h) (4u << (12 + 3 * (h)))
#define S_W6 (1u << 24)

#define STAMP(i)

#define KSTAMP(i)
template <class T> DEV T ldsr(unsigned a) { return *(const AS3 T*)(uintptr_t)(a); }
template <class T> DEV T ldsrv(unsigned a) { return *(const volatile AS3 T*)(uintptr_t)(a); }
template <class T> DEV void ldsw(unsigned a, T v) { *(AS3 T*)(uintptr_t)(a) = v; }
DEV u2 ldtr(unsigned a) { s4 v = __builtin_amdgcn_ds_read_tr16_b64_v4i16((AS3 s4*)(uintptr_t)(a)); return __builtin_bit_cast(u2, v); }
DEV int opaques(int x) { asm volatile("" : "+s"(x)); return x; }
DEV unsigned opaque(unsigned x) { asm volatile("" : "+v"(x)); return x; }

DEV unsigned pkbf(float a, float b) { f2 v = {a, b}; return __builtin_bit_cast(unsigned, __builtin_convertvector(v, b2)); }
DEV h2 cmul(h2 h, h2 e) { h2 t = h * (h2){e.x, e.x}; return __builtin_elementwise_fma((h2){-h.y, h.x}, (h2){e.y, e.y}, t); }
DEV h2 cmulc(h2 h, h2 e) { h2 t = h * (h2){e.x, e.x}; return __builtin_elementwise_fma((h2){h.y, -h.x}, (h2){e.y, e.y}, t); }
DEV f4 mfb(u4 a, u4 b, f4 c) { return __builtin_amdgcn_mfma_f32_16x16x32_bf16(__builtin_bit_cast(b8, a), __builtin_bit_cast(b8, b), c, 0, 0, 0); }
DEV f4 mfh(u4 a, u4 b, f4 c) { return __builtin_amdgcn_mfma_f32_16x16x32_f16(__builtin_bit_cast(h8, a), __builtin_bit_cast(h8, b), c, 0, 0, 0); }
DEV u4 cat(u2 a, u2 b) { return (u4){a.x, a.y, b.x, b.y}; }

#define TW(row, val) asm volatile("ds_write_addtid_b32 %0 offset:%1" :: "v"(val), "i"((row) * 288) : "memory")

DEV f2 csq(f2 z) {
    f2 t, r;
    asm("v_pk_mul_f32 %0, %1, %1 op_sel_hi:[0,1]" : "=v"(t) : "v"(z));
    asm("v_pk_fma_f32 %0, %1, %1, %2 op_sel:[1,1,0] op_sel_hi:[0,1,1] neg_lo:[1,0,0]" : "=v"(r) : "v"(z), "v"(t));
    return r;
}
DEV h2 pkh(f2 v) { return __builtin_convertvector(v, h2); }

DEV void arrive(unsigned a, unsigned v) {
    asm volatile("s_mov_b64 exec, 1\n ds_add_u32 %0, %1\n s_mov_b64 exec, -1" :: "v"(a), "v"(v) : "memory");
}
DEV void arrive4(unsigned a, unsigned v) {
    asm volatile("s_mov_b64 exec, 15\n ds_add_u32 %0, %1\n s_mov_b64 exec, -1" :: "v"(a), "v"(v) : "memory");
}
DEV void waitm(unsigned a, unsigned m) {
    CFENCE();
    while ((__builtin_amdgcn_readfirstlane(ldsrv<unsigned>(a)) & m) != m) {}
    CFENCE();
}

DEV void waits(unsigned a, unsigned m) {
    CFENCE();
    while ((__builtin_amdgcn_readfirstlane(ldsrv<unsigned>(a)) & m) != m) __builtin_amdgcn_s_sleep(1);
    CFENCE();
}
DEV h2 oph(h2 x) { asm volatile("" : "+v"(x)); return x; }

DEV void put4(unsigned a, unsigned p01, unsigned p23) {
    unsigned p12 = __builtin_amdgcn_alignbit(p23, p01, 16);
    ldsw<u2>(a, (u2){p01, p23});
    ldsw<unsigned short>(a + CS + 2, (unsigned short)p01);
    ldsw<unsigned>(a + CS + 4, p12);
    ldsw<unsigned short>(a + CS + 8, (unsigned short)(p23 >> 16));
    ldsw<unsigned>(a + 2 * CS + 4, p01);
    ldsw<unsigned>(a + 2 * CS + 8, p23);
    ldsw<unsigned short>(a + 3 * CS + 6, (unsigned short)p01);
    ldsw<unsigned>(a + 3 * CS + 8, p12);
    ldsw<unsigned short>(a + 3 * CS + 12, (unsigned short)(p23 >> 16));
}

DEV void put4g(unsigned a, unsigned p01, unsigned p23) {
    unsigned p12 = __builtin_amdgcn_alignbit(p23, p01, 16);
    ldsw<u2>(a + 3 * CS + 16, (u2){p01, p23});
    ldsw<unsigned short>(a + 2, (unsigned short)p01);
    ldsw<unsigned>(a + 4, p12);
    ldsw<unsigned short>(a + 16, (unsigned short)(p23 >> 16));
    ldsw<unsigned>(a + CS + 4, p01);
    ldsw<unsigned>(a + CS + 16, p23);
    ldsw<unsigned short>(a + 2 * CS + 6, (unsigned short)p01);
    ldsw<unsigned>(a + 2 * CS + 16, p12);
    ldsw<unsigned short>(a + 2 * CS + 20, (unsigned short)(p23 >> 16));
}

struct Ctx {
    unsigned uf;
    unsigned ut;
    unsigned wt;
    unsigned fa, ga;
    unsigned sw;
    unsigned bd;
};

DEV u4 ufrag(unsigned uf, int S, int s0) { return ldsr<u4>(uf + 512 * S + 2 * (266 - s0)); }
DEV u4 gfrag(unsigned ga, int ytop, int d) { return ldsr<u4>(ga + 4 * (ytop + 1 - 16 * d)); }
DEV u4 ffrag(unsigned fa, int i) { return cat(ldsrv<u2>(fa + 544 - 64 * i), ldsrv<u2>(fa + 552 - 64 * i)); }
DEV u2 cv4(f4 d) { return (u2){pkbf(d[0], d[1]), pkbf(d[2], d[3])}; }
DEV u4 utr(const Ctx& c, int S, int p1, int p2) { return cat(ldtr(c.ut + 512 * S + 2 * (266 - p1)), ldtr(c.ut + 512 * S + 2 * (266 - p2))); }
DEV u4 wtr(const Ctx& c, int S, int p1, int p2) { return cat(ldtr(c.wt + 512 * S + 2 * (20 - p1)), ldtr(c.wt + 512 * S + 2 * (20 - p2))); }
#define KA(d) gfrag(c.ga, 47, d)
#define KB(d) gfrag(c.ga, 175, d)
#define KC(d) gfrag(c.ga, 119, d)
#define KD(d) gfrag(c.ga, 247, d)
#define KH(d) gfrag(c.ga, 383, d)
#define VA(S) utr(c, S, 18, 6)
#define VB(S) utr(c, S, 10, 14)
#define VC(S) wtr(c, S, 4, 12)
#define VD(S) wtr(c, S, 8, 16)
#define BAND(i) ldsr<u4>(c.bd + 1024 * (i))
DEV u4 bx2(u4 a, u4 b1, u4 a2, u4 b2) {
    const f4 z = {0.f, 0.f, 0.f, 0.f};
    return cat(cv4(mfb(a, b1, z)), cv4(mfb(a2, b2, z)));
}
DEV u4 and4(u4 m, unsigned v) { return (u4){m[0] & v, m[1] & v, m[2] & v, m[3] & v}; }

template <int R> DEV void near8(const Ctx& c, const u4* F, f4& a0, f4& a1) {
    u4 f0[8], f1[8];
#pragma unroll
    for (int i = 0; i < 3; i++) { f0[i] = ufrag(c.uf, R, 10 + 32 * i); f1[i] = ufrag(c.uf, R + 1, 10 + 32 * i); }
    SB();
#pragma unroll
    for (int i = 3; i < 6; i++) { f0[i] = ufrag(c.uf, R, 10 + 32 * i); f1[i] = ufrag(c.uf, R + 1, 10 + 32 * i); }
#pragma unroll
    for (int i = 0; i < 3; i++) { a0 = mfb(F[i], f0[i], a0); a1 = mfb(F[i], f1[i], a1); }
    SB();
#pragma unroll
    for (int i = 6; i < 8; i++) { f0[i] = ufrag(c.uf, R, 10 + 32 * i); f1[i] = ufrag(c.uf, R + 1, 10 + 32 * i); }
#pragma unroll
    for (int i = 3; i < 6; i++) { a0 = mfb(F[i], f0[i], a0); a1 = mfb(F[i], f1[i], a1); }
    SB();
#pragma unroll
    for (int i = 6; i < 8; i++) { a0 = mfb(F[i], f0[i], a0); a1 = mfb(F[i], f1[i], a1); }
}
template <int R> DEV void near9(const Ctx& c, const u4* F, f4& a0, f4& a1) {
    u4 f0[9], f1[8];
#pragma unroll
    for (int i = 0; i < 3; i++) { f0[i] = ufrag(c.uf, R, 10 + 32 * i); f1[i] = ufrag(c.uf, R + 1, 10 + 32 * i); }
    SB();
#pragma unroll
    for (int i = 3; i < 6; i++) { f0[i] = ufrag(c.uf, R, 10 + 32 * i); f1[i] = ufrag(c.uf, R + 1, 10 + 32 * i); }
#pragma unroll
    for (int i = 0; i < 3; i++) { a0 = mfb(F[i], f0[i], a0); a1 = mfb(F[i], f1[i], a1); }
    a1 = mfb(F[8], f0[0], a1);
    SB();
#pragma unroll
    for (int i = 6; i < 9; i++) { f0[i] = ufrag(c.uf, R, 10 + 32 * i); if (i < 8) f1[i] = ufrag(c.uf, R + 1, 10 + 32 * i); }
#pragma unroll
    for (int i = 3; i < 6; i++) { a0 = mfb(F[i], f0[i], a0); a1 = mfb(F[i], f1[i], a1); }
    SB();
#pragma unroll
    for (int i = 6; i < 9; i++) { a0 = mfb(F[i], f0[i], a0); if (i < 8) a1 = mfb(F[i], f1[i], a1); }
}
template <int S> DEV void wbuild(const Ctx& c, u4 B1, unsigned ww) {
    const f4 z = {0.f, 0.f, 0.f, 0.f};
    const u4 n0 = ufrag(c.uf, S, 10);
    u4 n1;
    if (S < 6) n1 = ufrag(c.uf, S + 1, 10);
    ldsw<u2>(ww + 512 * S, cv4(mfb(B1, n0, z)));
    if (S < 6) ldsw<u2>(ww + 512 * (S + 1), cv4(mfb(B1, n1, z)));
}

#define PROF_OUT()
#define STG(pp) (((pp) >> 1) * 8 * T_STRIDE + ((pp) & 1) * 2048)
#define STAGE(pp, x0, x1) do { ldsw<u2>(st + ((pp) & 1) * 2048, cv4(x0)); ldsw<u2>(st + ((pp) & 1) * 2048 + 576, cv4(x1)); \
    arrive(cnt, 1u << (3 * (pp))); } while (0)
#define FINISH() do { PROF_OUT(); asm volatile("s_endpgm" ::: "memory"); } while (0)

__global__ __launch_bounds__(1024, 4) void s4d_kernel(const unsigned short* __restrict__ u, const unsigned short* __restrict__ lar,
                                                      const unsigned short* __restrict__ aim, const unsigned short* __restrict__ cre,
                                                      const unsigned short* __restrict__ cim, const unsigned short* __restrict__ ldt,
                                                      const unsigned short* __restrict__ dsk, unsigned short* __restrict__ out
) {
    __shared__ __attribute__((aligned(16))) char smem[LDS_BYTES];
    const unsigned sb = (unsigned)(uintptr_t)(AS3 char*)smem;
    STAMP(0);
    const int tid = threadIdx.x;
    l8 ka;
    unsigned cpf;
    {
        unsigned long long pc;
        asm volatile("s_load_dwordx16 %0, %2, 0x0\n"
                     "s_getpc_b64 %1" : "=&s"(ka), "=&s"(pc) : "s"(__builtin_amdgcn_kernarg_segment_ptr()));
        asm volatile("v_lshlrev_b32 %0, 7, %2\n"
                     "v_min_u32 %0, %3, %0\n"
                     "global_load_dword %0, %0, %1"
                     : "=&v"(cpf) : "s"(pc), "v"(tid), "i"(CODE_BYTES));
    }
    SB();
    const int lane = tid & 63;
    const int w = __builtin_amdgcn_readfirstlane(tid >> 6);
    const int c4 = w & 3, r = w >> 2;
    const int bx = blockIdx.x;
    const int xi = bx >> 3;
    const int gcnt = (int)(0x1818140c0c141818ull >> (8 * (bx & 7))) & 0xff, gb = (int)(0x88705c5044301800ull >> (8 * (bx & 7))) & 0xff;
    if (xi >= gcnt) { asm volatile("s_waitcnt vmcnt(0) lgkmcnt(0)\n s_endpgm" :: "v"(cpf), "s"(ka) : "memory"); }
    const int grp = gb + xi;
    const int ch = grp * 4 + c4;
    const int la = lane & 15, lg = lane >> 4;

    const unsigned ub = sb + U_OFF + c4 * U_STRIDE;
    const unsigned fgb = sb + FG_OFF + c4 * FG_STRIDE;
    const f4 zf = {0.f, 0.f, 0.f, 0.f};
    Ctx c;
#define COMMON_SETUP() \
    c.uf = opaque(ub + 32 * la + 16 * lg); \
    c.ut = opaque(ub + 8 * lane); \
    c.wt = opaque(sb + W_OFF + c4 * W_STRIDE + 8 * lane); \
    c.fa = opaque(fgb + F_REL + (CS + 2) * ((la + 3) & 3) - 2 * la + 16 * lg + 2); \
    c.ga = opaque(fgb + CS * (la & 3) - 4 * (la & 12) + 16 * lg); \
    c.bd = opaque(sb + B_OFF + 16 * lane);
    unsigned pl, pa, pr, pi, ldh, dkh, uo;
    const u4 z4 = {0u, 0u, 0u, 0u};
    const unsigned za = opaque(sb + FG_OFF + tid * 16);
    if (r == 3) {
        const int lt = tid & 255;
        ldsw<u4>(za + 12288, z4);
        ldsw<u4>(za + 16384 - 960, z4);
        const unsigned zj = min((unsigned)lt >> 2, 39u);
        const unsigned z1 = opaque(U_OFF + zj * 16), z2 = opaque(U_OFF + zj * 1024 + (528 - 33 * 1024)), z3 = opaque(W_OFF + (zj - 38) * 16);
        ldsw<u4>(sb + (lt & 3) * (zj < 38 ? U_STRIDE : W_STRIDE) + (zj < 34 ? z1 : (zj < 38 ? z2 : z3)), z4);
        SB();
        const unsigned idx = min(lt * gcnt + xi, 4095);
        const unsigned poff = opaque((idx >> 1) * 1280 + gb * 8 + (idx & 1) * (gcnt * 8 - 4));
        uo = opaque(lt * 2560 + grp * 8);
        KILL(pa); KILL(pr); KILL(pi); KILL(ldh); KILL(dkh);
        asm volatile("s_waitcnt lgkmcnt(0)" :: "s"(ka) : "memory");
        asm volatile("global_load_dword %0, %1, %2" : "=v"(pl) : "v"(poff), "s"(ka[0]));
    } else {
        ldsw<u4>(za, z4); ldsw<u4>(za + 12288, z4);
        const unsigned vc = opaque(2 * ch), vo = opaque(2 * (ch * NN + r * 64 + lane));
        if (w < 6) {
            const unsigned pat = (unsigned)((w < 4 ? 0x3F801F00001F00E0ull : 0xC07FC0ull) >> (16 * c4)) & 0xffffu;
            const unsigned val = w == 5 ? 0xffffu : 0x3F80u;
            const unsigned by = (pat << la >> (8 * lg)) & 0xffu;
            const unsigned e = by | (by << 15);
            u4 bq;
#pragma unroll
            for (int m = 0; m < 4; m++) bq[m] = __umul24((e >> (2 * m)) & 0x10001u, val);
            ldsw<u4>(za + (B_OFF - FG_OFF), bq);
        }
        ldh = opaque(0u); pl = opaque(0u); pa = opaque(0u); pr = opaque(0u); pi = opaque(0u); dkh = opaque(0u);
        SB();
        asm volatile("s_waitcnt lgkmcnt(0)" :: "s"(ka) : "memory");
        asm volatile("global_load_short_d16_hi %0, %1, %2" : "+v"(ldh) : "v"(vc), "s"(ka[5]));
        asm volatile("global_load_short_d16_hi %0, %1, %2" : "+v"(pl) : "v"(vo), "s"(ka[1]));
        asm volatile("global_load_short_d16_hi %0, %1, %2" : "+v"(pa) : "v"(vo), "s"(ka[2]));
        asm volatile("global_load_short_d16_hi %0, %1, %2" : "+v"(pr) : "v"(vo), "s"(ka[3]));
        asm volatile("global_load_short_d16_hi %0, %1, %2" : "+v"(pi) : "v"(vo), "s"(ka[4]));
        asm volatile("global_load_short_d16_hi %0, %1, %2" : "+v"(dkh) : "v"(vc), "s"(ka[6]));
        KILL(uo);
    }
    STAMP(1);
    asm volatile("s_barrier" ::: "memory");
    STAMP(2);
    SB();
    if (opaques(r) == 3) {
        __builtin_amdgcn_s_setprio(3);
        u2 ua[2], uc[2], ud[2];
#define ULOAD(x) \
        asm volatile("global_load_dwordx2 %0, %1, %2" : "=v"(x[0]) : "v"(uo), "s"(ka[0])); \
        asm volatile("global_load_dwordx2 %0, %1, %2 offset:1280" : "=v"(x[1]) : "v"(uo), "s"(ka[0])); \
        uo += 512 * 1280;
#define UW(cc, h, val) asm volatile("ds_write_addtid_b32 %0 offset:%1" :: "v"(val), "i"((cc) * U_STRIDE + 1024 * (h)) : "memory")
#define UPUT(x, h, n) \
        asm volatile("s_waitcnt vmcnt(" #n ")" : "+v"(x[0]), "+v"(x[1])); \
        UW(0, h, __builtin_amdgcn_perm(x[1][0], x[0][0], 0x05040100u)); \
        UW(1, h, __builtin_amdgcn_perm(x[1][0], x[0][0], 0x07060302u)); \
        UW(2, h, __builtin_amdgcn_perm(x[1][1], x[0][1], 0x05040100u)); \
        UW(3, h, __builtin_amdgcn_perm(x[1][1], x[0][1], 0x07060302u)); \
        arrive4(sw4, 1u << (12 + 3 * (h))); SB();
        ULOAD(ua);
        ULOAD(uc);
        SB();
        c.uf = opaque(ub + 32 * la + 16 * lg);
        c.sw = opaque(sb + MISC_OFF + 4 * c4);
        c.bd = opaque(sb + B_OFF + 16 * lane);
        const unsigned sw4 = opaque(sb + MISC_OFF + 4 * lane);
        const unsigned ww = opaque(sb + W_OFF + 32 + c4 * W_STRIDE + 32 * la + 8 * lg);
        const u4 B1 = BAND(0);
        asm volatile("s_mov_b32 m0, %0\n s_nop 0" :: "s"(sb + U_OFF + 532 + 256 * c4) : "memory");
        SB();
        ULOAD(ud);
        STAMP(3);
        UPUT(ua, 0, 4);
        asm volatile("" :: "v"(pl), "v"(cpf));
        STAMP(4);
        ULOAD(ua); SB();
        UPUT(uc, 1, 4);
        waitm(c.sw, S_U(0)); wbuild<0>(c, B1, ww); arrive(c.sw, S_W01); SB();
        UPUT(ud, 2, 2);
        STAMP(5);
        waitm(c.sw, S_U(1)); wbuild<2>(c, B1, ww); arrive(c.sw, S_W23); SB();
        UPUT(ua, 3, 0);
        STAMP(6);
        waitm(c.sw, S_U(2)); wbuild<4>(c, B1, ww); arrive(c.sw, S_W45); SB();
        waitm(c.sw, S_U(3)); wbuild<6>(c, B1, ww); arrive(c.sw, S_W6);
        STAMP(7);
        FINISH();
        KILL(cpf); KILL(pl); KILL(pa); KILL(pr); KILL(pi); KILL(ldh); KILL(dkh);
    }
    {
        __builtin_amdgcn_s_setprio(3);
        const unsigned tb = sb + w * T_STRIDE;
        const unsigned tr = opaque(tb + 288 * la + 16 * lg);
        const unsigned pw = opaque(sb + P_OFF + c4 * 6144 + 16 * lane);
        const unsigned pw0 = opaque(pw + (r >> 1) * 1024), pw2 = opaque(pw + (r & 1) * 1024);
        unsigned wk, wpt, st;
        c.sw = opaque(sb + MISC_OFF + 4 * c4);
        if (r == 1) {
            const unsigned u0 = __builtin_amdgcn_perm(0x6D492D48u, 0x6C092C08u, la & 7), u1 = __builtin_amdgcn_perm(0x24406401u, 0x25654100u, la & 7);
            wk = opaque(fgb + 8 * ((la < 8 ? u0 : u1) & 0xffu) + 16 * lg);
            const unsigned wa = opaque((CS + 2) * (lane & 3) + F_REL + 54 - 2 * (la >> 2)), wb = opaque(CS * (lane & 3) + ((lane & 3) == 3 ? 80 : 66 + 2 * (lane & 3)));
            wpt = opaque(fgb + (la < 12 ? wa : wb));
            KILL(st); KILL(c.uf); KILL(c.ut); KILL(c.wt); KILL(c.fa); KILL(c.ga); KILL(c.bd);
        } else {
            COMMON_SETUP();
            st = opaque(tb + 16 * c4 + 8 * la + 128 * lg + 64 * (lg >> 1));
            const unsigned u0 = __builtin_amdgcn_perm(0x80889098u, 0x5D1D5C1Cu, la & 7), u1 = __builtin_amdgcn_perm(0u, 0x81899199u, la & 3);
            const unsigned w2 = opaque(8 * ((la < 8 ? u0 : u1) & 0xffu) + 16 * lg), w0 = opaque(F_REL + 56 + 32 * la + 8 * lg);
            wk = opaque(fgb + (r == 0 ? w0 : w2));
            KILL(wpt);
        }
        const unsigned cnt = opaque(sb + MISC_OFF + 16);
        asm volatile("s_mov_b32 m0, %0\n s_nop 0" :: "s"(tb) : "memory");
        STAMP(3);
        asm volatile("s_waitcnt vmcnt(0)" : "+v"(ldh), "+v"(pl), "+v"(pa), "+v"(pr), "+v"(pi), "+v"(dkh) : "v"(cpf));
        const float LOG2E = 1.4426950408889634f;
        const float dt = __uint_as_float(pkbf(0.f, __builtin_amdgcn_exp2f(__uint_as_float(ldh) * LOG2E)));
        const float arp = __uint_as_float(pkbf(0.f, __builtin_amdgcn_exp2f(__uint_as_float(pl) * LOG2E)));
        const float m1 = arp * (-dt * LOG2E);
        const float th = __builtin_amdgcn_fractf(__uint_as_float(pa) * (dt * 0.15915494309189535f));
        const float mg = __builtin_amdgcn_exp2f(m1);
        f2 z = (f2){__builtin_amdgcn_cosf(th), __builtin_amdgcn_sinf(th)} * mg;
        STAMP(4);
        unsigned mx;
        asm("v_max_f32 %0, |%1|, |%2|\n s_nop 1" : "=v"(mx) : "v"(pr), "v"(pi));
#define MXS(ctl, rm) mx = max(mx, (unsigned)__builtin_amdgcn_update_dpp(0, (int)mx, ctl, rm, 0xf, true))
        const h2 e1 = pkh(z); MXS(0x111, 0xf); z = csq(z); SB();
        const h2 e2 = pkh(z); MXS(0x112, 0xf); z = csq(z); SB();
        const h2 e4 = pkh(z); MXS(0x114, 0xf); z = csq(z); SB();
        const h2 e8 = pkh(z);
        const h2 one = {(_Float16)1.f, (_Float16)0.f};
        h2 t0, t1, t2, t3;
        t0 = cmul(e2, e1); MXS(0x118, 0xf); t1 = cmul(e4, e1); TW(15, one); TW(14, e1); SB();
        const h2 y6 = cmul(e4, e2); MXS(0x142, 0xa); t2 = cmul(e8, e1); TW(13, e2); TW(11, e4); TW(7, e8); SB();
        const h2 y10 = cmul(e8, e2); MXS(0x143, 0xc); const h2 y12 = cmul(e8, e4); TW(12, t0); TW(10, t1); SB();
        const float th2 = th * 16.f;
        const float mg2 = __builtin_amdgcn_exp2f(m1 * 16.f);
        t0 = cmul(y6, e1); const h2 y14 = cmul(e8, y6); TW(9, y6); TW(6, t2); SB();
        z = (f2){__builtin_amdgcn_cosf(th2), __builtin_amdgcn_sinf(th2)} * mg2;
        const unsigned ex = max((unsigned)__builtin_amdgcn_readlane(mx, 63) >> 23, 16u);
        const float sc = __uint_as_float((268u - ex) << 23);
        const float ks = dt * __uint_as_float((ex - 13u) << 23);
        t1 = cmul(y10, e1); t2 = cmul(y12, e1); TW(5, y10); TW(3, y12); SB();
        t3 = cmul(y14, e1);
        TW(8, t0); TW(1, y14); SB();
        const h2 x0 = pkh((f2){__uint_as_float(pr), -__uint_as_float(pi)} * sc);
        const h2 e16 = pkh(z); z = csq(z);
        TW(4, t1); TW(2, t2); TW(0, t3);
        u4 yf[4];
#pragma unroll
        for (int s = 0; s < 4; s++) yf[s] = ldsr<u4>(tr + 64 * s);
        const h2 e32 = pkh(z); z = csq(z);
        SB();
        const h2 x1 = cmulc(x0, e16), x2 = cmulc(x0, e32);
        const h2 e64 = pkh(z); z = csq(z);
        TW(15, x0); SB();
        const h2 x3 = cmulc(x1, e32), x4 = cmulc(x0, e64);
        const h2 e128 = pkh(z); SB();
        const h2 x5 = cmulc(x1, e64), x6 = cmulc(x2, e64); TW(14, x1); TW(13, x2); SB();
        const h2 x7 = cmulc(x3, e64); t0 = cmulc(x0, e128); TW(12, x3); TW(11, x4); SB();
        t1 = cmulc(x1, e128); t2 = cmulc(x2, e128); TW(10, x5); TW(9, x6); SB();
        t3 = cmulc(x3, e128); h2 t4 = cmulc(x4, e128); TW(8, x7); TW(7, t0); SB();
        t0 = cmulc(x5, e128); h2 t5 = cmulc(x6, e128); TW(6, t1); TW(5, t2); SB();
        t1 = cmulc(x7, e128); TW(4, t3); TW(3, t4); SB();
        z = csq(z); TW(2, t0); TW(1, t5); SB();
        TW(0, t1);
        u4 xf[4];
#pragma unroll
        for (int s = 0; s < 4; s++) xf[s] = ldsr<u4>(tr + 64 * s);
        const h2 e256 = pkh(z); z = csq(z);
        const h2 p272 = cmul(e256, e16); t0 = cmul(e256, e4);
        SB();
        t1 = cmul(e256, y6); const h2 p264 = cmul(e256, e8); SB();
        f4 acc0 = zf, acc1 = zf, acc2 = zf;
#pragma unroll
        for (int s = 0; s < 4; s++) acc0 = mfh(yf[s], xf[s], acc0);
        t2 = cmul(e256, y10); t3 = cmul(e256, y12); TW(7, p272); TW(1, t0); SB();
        t4 = cmul(e256, y14); t5 = cmul(p272, e2); TW(2, t1); TW(3, p264); SB();
        const h2 e512 = pkh(z); z = csq(z);
        acc0 *= ks;
        STAMP(5);
        asm volatile("s_cmp_eq_u32 %0, 0\n s_cbranch_scc1 .Lp0_%=\n ds_write_b128 %1, %2\n s_mov_b64 exec, 1\n ds_add_u32 %3, %4\n s_mov_b64 exec, -1\n"
                     ".Lp0_%=:\n s_cmp_eq_u32 %0, 2\n s_cbranch_scc1 .Lp1_%=\n s_setprio 2\n .Lp1_%=:"
                     :: "s"(r), "v"(pw0), "v"(acc0), "v"(c.sw), "v"(1u) : "scc", "memory");
        SB();
        const h2 p520 = cmul(e512, e8), p528 = cmul(e512, e16); TW(4, t2); TW(5, t3); SB();
        const h2 p516 = cmul(e512, e4), p524 = cmul(e512, y12); TW(6, t4); TW(0, t5); SB();
        const h2 e1024 = pkh(z);
        t4 = cmul(e256, e2); TW(12, p520); TW(8, p528); TW(9, e256); SB();
        const h2 p776 = cmul(e256, p520), p784 = cmul(e256, p528); TW(15, p516); TW(11, p524); TW(10, t4);
        SB();
#pragma unroll
        for (int s = 0; s < 4; s++) yf[s] = ldsr<u4>(tr + 64 * s);
        t0 = cmul(e256, p516); t1 = cmul(e256, p524);
        t2 = cmul(e1024, e8); t3 = cmul(e1024, p264); TW(1, p776); TW(3, p784); SB();
#pragma unroll
        for (int s = 0; s < 4; s++) acc1 = mfh(xf[s], yf[s], acc1);
        t4 = cmul(e1024, p520); t5 = cmul(e1024, p776); TW(0, t0); TW(2, t1); SB();
        t0 = cmul(e1024, e16); t1 = cmul(e1024, p272); TW(4, t2); TW(5, t3); SB();
        t2 = cmul(e1024, p528); t3 = cmul(e1024, p784); TW(6, t4); TW(7, t5); SB();
        TW(8, t0); TW(9, t1); TW(10, t2); TW(11, t3);
        SB();
#pragma unroll
        for (int s = 0; s < 4; s++) yf[s] = ldsr<u4>(tr + 64 * s);
        acc1 *= ks;
        SB();
        if (r != 1) { ldsw<f4>(pw0 + 2048, acc1); arrive(c.sw, 4u); }
        KSTAMP(8);
        SB();
#pragma unroll
        for (int s = 0; s < 4; s++) acc2 = mfh(xf[s], yf[s], acc2);
        acc2 *= ks;
        if (r == 2) {
            waitm(c.sw, S_C2);
            f4 kk = acc2 + ldsr<f4>(pw + 4096) + ldsr<f4>(pw + 5120);
            const unsigned q01 = pkbf(kk[0], kk[1]), q23 = pkbf(kk[2], kk[3]);
            if (la < 12) put4g(wk, q01, q23);
            const unsigned k1024 = (__builtin_amdgcn_readlane(q01, 3) & 0xffffu) * 0x10001u;
            arrive(c.sw, S_G2);
            __builtin_amdgcn_s_setprio(1);
            STAMP(6);
            f4 a[4] = {zf, zf, zf, zf};
            u4 p[5], v[5], k[4];
            {
                const u4 B3 = BAND(3), B4 = BAND(4), BF = and4(BAND(5), k1024);
#pragma unroll
                for (int d = 0; d < 4; d++) k[d] = KH(4 + d);
                waits(c.sw, S_U(0) | S_U(1));
                u4 q[4], n[4];
#pragma unroll
                for (int S = 0; S < 4; S++) { p[S] = ufrag(c.uf, S, 18); q[S] = ufrag(c.uf, S, 26); }
#pragma unroll
                for (int S = 0; S < 4; S++) n[S] = ufrag(c.uf, S, 10);
                SB();
#pragma unroll
                for (int S = 0; S < 4; S++) v[S] = bx2(p[S], B3, q[S], B4);
#pragma unroll
                for (int t = 0; t < 4; t++) a[t] = mfb(BF, n[t], a[t]);
            }
            const u4 B2a = BAND(1), B2b = BAND(2);
            SB();
#pragma unroll
            for (int S = 0; S < 4; S++)
#pragma unroll
                for (int t = S; t < 4; t++) a[t] = mfb(v[S], k[t - S], a[t]);
#pragma unroll
            for (int S = 0; S < 4; S++) v[S] = bx2(p[S], B2b, p[S], B2a);
            waits(c.sw, S_G1 | S_W45);
            STAMP(7);
            p[4] = ufrag(c.uf, 4, 18);
            const u4 kd3 = KD(3), kd4 = KD(4);
            u4 vc[6];
#pragma unroll
            for (int S = 0; S < 6; S++) vc[S] = VC(S);
#pragma unroll
            for (int d = 0; d < 4; d++) k[d] = KC(4 - d);
            SB();
            v[4] = bx2(p[4], B2b, p[4], B2a);
#pragma unroll
            for (int t = 0; t < 4; t++) a[t] = mfb(v[t], kd4, a[t]);
#pragma unroll
            for (int t = 0; t < 4; t++) a[t] = mfb(v[t + 1], kd3, a[t]);
            SB();
            u4 k1[4], k2;
#pragma unroll
            for (int S = 0; S < 4; S++) v[S] = VA(2 + S);
            k2 = KA(2); k1[0] = KA(1);
#pragma unroll
            for (int d = 0; d < 4; d++)
#pragma unroll
                for (int t = 0; t < 4; t++) if (t + d < 6) a[t] = mfb(vc[t + d], k[d], a[t]);
            k1[2] = k[3];
            SB();
#pragma unroll
            for (int S = 0; S < 4; S++) p[S] = VB(2 + S);
            k[0] = KB(2); k1[1] = KB(1);
#pragma unroll
            for (int t = 0; t < 4; t++) a[t] = mfb(v[t], k2, a[t]);
#pragma unroll
            for (int t = 0; t < 3; t++) a[t] = mfb(v[t + 1], k1[0], a[t]);
            SB();
#pragma unroll
            for (int S = 0; S < 4; S++) v[S] = VD(2 + S);
            k2 = KD(2); k1[3] = KD(1);
#pragma unroll
            for (int t = 0; t < 4; t++) a[t] = mfb(p[t], k[0], a[t]);
#pragma unroll
            for (int t = 0; t < 3; t++) a[t] = mfb(p[t + 1], k1[1], a[t]);
            SB();
            waitm(c.sw, S_F);
            u4 F[9];
#pragma unroll
            for (int i = 0; i < 9; i++) F[i] = ffrag(c.fa, i);
#pragma unroll
            for (int t = 0; t < 4; t++) a[t] = mfb(v[t], k2, a[t]);
#pragma unroll
            for (int t = 0; t < 3; t++) a[t] = mfb(v[t + 1], k1[3], a[t]);
            STAMP(8);
            near9<4>(c, F, a[0], a[1]);
            STAGE(2, a[0], a[1]);
            STAMP(9);
            waitm(c.sw, S_W6);
            near9<6>(c, F, a[2], a[3]);
            {
                const u4 va = VA(6), vb = VB(6), vc6 = VC(6), vd = VD(6);
                a[3] = mfb(va, k1[0], a[3]); a[3] = mfb(vb, k1[1], a[3]); a[3] = mfb(vc6, k1[2], a[3]); a[3] = mfb(vd, k1[3], a[3]);
            }
            STAGE(3, a[2], a[3]);
            STAMP(10);
            FINISH();
            KILL(acc0); KILL(acc1); KILL(acc2); KILL(wk); KILL(wpt); KILL(dkh);
        }
        ldsw<f4>(pw2 + 4096, acc2);
        arrive(c.sw, 16u);
        __builtin_amdgcn_s_setprio(3);
        KSTAMP(7);
        if (r == 1) {
            waitm(c.sw, S_C1);
            f4 kk = acc1 + ldsr<f4>(pw + 2048) + ldsr<f4>(pw + 3072);
            unsigned q01 = pkbf(kk[0], kk[1]), q23 = pkbf(kk[2], kk[3]);
            const unsigned k256 = __builtin_amdgcn_readlane(q23, 57) >> 16;
            const unsigned k258 = __builtin_amdgcn_readlane(q23, 58) >> 16;
            const unsigned k512 = __builtin_amdgcn_readlane(q01, 7);
            q01 = __builtin_amdgcn_update_dpp(q01, q01, 0xC0, 0xf, 0xc, false);
            q23 = __builtin_amdgcn_update_dpp(q23, q23, 0xC0, 0xf, 0xc, false);
            put4g(wk, q01, q23);
            CFENCE();
            ldsw<unsigned short>(wpt, (unsigned short)(la < 8 ? k256 : (la < 12 ? k258 : k512)));
            arrive(c.sw, S_G1);
            STAMP(6);
            const unsigned ea = opaque(sb + ((lane >> 2) & 3) * (T_STRIDE + 16) + 8 * lg + 128 * (lane & 3) + 32 * (lane & 2) + 64 * (c4 & 1) + 576 * (c4 >> 1));
            auto ro = __builtin_amdgcn_make_buffer_rsrc((void*)ka[7], (short)0, 2048 * 1280, 0x00020000);
            const unsigned vo = opaque((lane + 128 * c4) * 1280 + grp * 8);
#pragma unroll
            for (int pp = 0; pp < 4; pp++) {
                waits(cnt, 4u << (3 * pp));
                const u2 v0 = ldtr(ea + STG(pp)), v1 = ldtr(ea + STG(pp) + 32);
                __builtin_amdgcn_raw_buffer_store_b64(v0, ro, vo, 512 * pp * 1280, 0);
                __builtin_amdgcn_raw_buffer_store_b64(v1, ro, vo, (512 * pp + 64) * 1280, 0);
                STAMP(7 + pp);
            }
            FINISH();
            KILL(acc0); KILL(wk); KILL(dkh);
        }
        {
            waitm(c.sw, S_C0);
            f4 kk = acc0 + ldsr<f4>(pw) + ldsr<f4>(pw + 1024);
            kk[3] += lane == 63 ? __uint_as_float(dkh) : 0.f;
            put4(wk, pkbf(kk[0], kk[1]), pkbf(kk[2], kk[3]));
            arrive(c.sw, S_F);
            __builtin_amdgcn_s_setprio(XPRIO);
            STAMP(6);
            f4 a[4] = {zf, zf, zf, zf};
            u4 F[8];
#pragma unroll
            for (int i = 0; i < 8; i++) F[i] = ffrag(c.fa, i);
            waits(c.sw, S_U(0));
            STAMP(7);
            near8<0>(c, F, a[0], a[1]);
            waits(c.sw, S_U(1));
            near8<2>(c, F, a[2], a[3]);
            u4 n[4];
#pragma unroll
            for (int t = 0; t < 4; t++) n[t] = ufrag(c.uf, t, 266);
            const u4 va0 = VA(0), vb0 = VB(0);
            waits(c.sw, S_G1 | S_W01);
            STAMP(8);
            __builtin_amdgcn_s_setprio(2);
            const u4 F8 = ffrag(c.fa, 8), ka1 = KA(1), kb1 = KB(1), vc0 = VC(0), kc1 = KC(1), vd0 = VD(0), kd1 = KD(1);
            SB();
            const u4 va1 = VA(1), vb1 = VB(1), vc1 = VC(1), vd1 = VD(1), ka2 = KA(2), kb2 = KB(2), kc2 = KC(2), kd2 = KD(2);
            a[1] = mfb(va0, ka1, a[1]); a[0] = mfb(F8, n[0], a[0]); a[1] = mfb(vb0, kb1, a[1]); a[1] = mfb(F8, n[1], a[1]);
            a[1] = mfb(vc0, kc1, a[1]); a[1] = mfb(vd0, kd1, a[1]);
            a[2] = mfb(F8, n[2], a[2]); a[3] = mfb(F8, n[3], a[3]);
            STAGE(0, a[0], a[1]);
            STAMP(9);
            const u4 B2a = BAND(1), B2b = BAND(2), p0 = ufrag(c.uf, 0, 18), va2 = VA(2), vb2 = VB(2);
            a[2] = mfb(va1, ka1, a[2]); a[3] = mfb(va1, ka2, a[3]); a[2] = mfb(va0, ka2, a[2]);
            a[2] = mfb(vb1, kb1, a[2]); a[3] = mfb(vb1, kb2, a[3]); a[2] = mfb(vb0, kb2, a[2]);
            waitm(c.sw, S_G2 | S_W23);
            const u4 vc2 = VC(2), vd2 = VD(2), kc3 = KC(3), kd3 = KD(3);
            const u4 w0 = bx2(p0, B2b, p0, B2a);
            a[2] = mfb(vc1, kc1, a[2]); a[3] = mfb(vc1, kc2, a[3]); a[2] = mfb(vc0, kc2, a[2]);
            a[2] = mfb(vd1, kd1, a[2]); a[3] = mfb(vd1, kd2, a[3]); a[2] = mfb(vd0, kd2, a[2]);
            a[3] = mfb(va2, ka1, a[3]); a[3] = mfb(vb2, kb1, a[3]); a[3] = mfb(vc2, kc1, a[3]); a[3] = mfb(vd2, kd1, a[3]);
            a[3] = mfb(vc0, kc3, a[3]); a[3] = mfb(w0, kd3, a[3]);
            STAGE(1, a[2], a[3]);
            STAMP(10);
            FINISH();
        }
    }
}

extern "C" void kernel_launch(void* const* d_in, const int* in_sizes, int n_in, void* d_out, int out_size, void* d_ws, size_t ws_size,
                              hipStream_t stream) {
    s4d_kernel<<<192, 1024, 0, stream>>>((const unsigned short*)d_in[0], (const unsigned short*)d_in[1], (const unsigned short*)d_in[2],
                                         (const unsigned short*)d_in[3], (const unsigned short*)d_in[4], (const unsigned short*)d_in[5],
                                         (const unsigned short*)d_in[6], (unsigned short*)d_out
    );
}
```

```cpp
#include <hip/hip_runtime.h>
#include <stdint.h>

typedef _Float16 h2 __attribute__((ext_vector_type(2)));
typedef _Float16 h8 __attribute__((ext_vector_type(8)));
typedef __bf16 b2 __attribute__((ext_vector_type(2)));
typedef __bf16 b8 __attribute__((ext_vector_type(8)));
typedef float f2 __attribute__((ext_vector_type(2)));
typedef float f4 __attribute__((ext_vector_type(4)));
typedef unsigned u2 __attribute__((ext_vector_type(2)));
typedef unsigned u4 __attribute__((ext_vector_type(4)));
typedef unsigned long long l8 __attribute__((ext_vector_type(8)));
typedef short s4 __attribute__((ext_vector_type(4)));
#define AS3 __attribute__((address_space(3)))
#define DEV __device__ __forceinline__
#define SB() __builtin_amdgcn_sched_barrier(0)
#define KILL(x) asm volatile("" : "=v"(x))
#define CFENCE() asm volatile("" ::: "memory")

#define NN 192
#define XPRIO 1
#define CODE_BYTES 12288

#define T_STRIDE 4608
#define U_OFF 55296
#define U_STRIDE 4640
#define FG_OFF 73856
#define CS 1984
#define FG_STRIDE (4 * CS)
#define F_REL 1360
#define MISC_OFF (FG_OFF + 4 * FG_STRIDE)
#define W_OFF (MISC_OFF + 64)
#define W_STRIDE 3648
#define P_OFF (W_OFF + 4 * W_STRIDE)
#define B_OFF (P_OFF + 4 * 6144)
#define LDS_BYTES (B_OFF + 6144)
#define S_C0 2u
#define S_C1 8u
#define S_C2 32u
#define S_F 64u
#define S_G1 128u
#define S_G2 256u
#define S_W01 512u
#define S_W23 1024u
#define S_W45 2048u
#define S_U(h) (4u << (12 + 3 * (h)))
#define S_W6 (1u << 24)

#define STAMP(i)

#define KSTAMP(i)
template <class T> DEV T ldsr(unsigned a) { return *(const AS3 T*)(uintptr_t)(a); }
template <class T> DEV T ldsrv(unsigned a) { return *(const volatile AS3 T*)(uintptr_t)(a); }
template <class T> DEV void ldsw(unsigned a, T v) { *(AS3 T*)(uintptr_t)(a) = v; }
DEV u2 ldtr(unsigned a) { s4 v = __builtin_amdgcn_ds_read_tr16_b64_v4i16((AS3 s4*)(uintptr_t)(a)); return __builtin_bit_cast(u2, v); }
DEV int opaques(int x) { asm volatile("" : "+s"(x)); return x; }
DEV unsigned opaque(unsigned x) { asm volatile("" : "+v"(x)); return x; }

DEV unsigned pkbf(float a, float b) { f2 v = {a, b}; return __builtin_bit_cast(unsigned, __builtin_convertvector(v, b2)); }
DEV h2 cmul(h2 h, h2 e) { h2 t = h * (h2){e.x, e.x}; return __builtin_elementwise_fma((h2){-h.y, h.x}, (h2){e.y, e.y}, t); }
DEV h2 cmulc(h2 h, h2 e) { h2 t = h * (h2){e.x, e.x}; return __builtin_elementwise_fma((h2){h.y, -h.x}, (h2){e.y, e.y}, t); }
DEV f4 mfb(u4 a, u4 b, f4 c) { return __builtin_amdgcn_mfma_f32_16x16x32_bf16(__builtin_bit_cast(b8, a), __builtin_bit_cast(b8, b), c, 0, 0, 0); }
DEV f4 mfh(u4 a, u4 b, f4 c) { return __builtin_amdgcn_mfma_f32_16x16x32_f16(__builtin_bit_cast(h8, a), __builtin_bit_cast(h8, b), c, 0, 0, 0); }
DEV u4 cat(u2 a, u2 b) { return (u4){a.x, a.y, b.x, b.y}; }

#define TW(row, val) asm volatile("ds_write_addtid_b32 %0 offset:%1" :: "v"(val), "i"((row) * 288) : "memory")

DEV f2 csq(f2 z) {
    f2 t, r;
    asm("v_pk_mul_f32 %0, %1, %1 op_sel_hi:[0,1]" : "=v"(t) : "v"(z));
    asm("v_pk_fma_f32 %0, %1, %1, %2 op_sel:[1,1,0] op_sel_hi:[0,1,1] neg_lo:[1,0,0]" : "=v"(r) : "v"(z), "v"(t));
    return r;
}
DEV h2 pkh(f2 v) { return __builtin_convertvector(v, h2); }

DEV void arrive(unsigned a, unsigned v) {
    asm volatile("s_mov_b64 exec, 1\n ds_add_u32 %0, %1\n s_mov_b64 exec, -1" :: "v"(a), "v"(v) : "memory");
}
DEV void arrive4(unsigned a, unsigned v) {
    asm volatile("s_mov_b64 exec, 15\n ds_add_u32 %0, %1\n s_mov_b64 exec, -1" :: "v"(a), "v"(v) : "memory");
}
DEV void waitm(unsigned a, unsigned m) {
    CFENCE();
    while ((__builtin_amdgcn_readfirstlane(ldsrv<unsigned>(a)) & m) != m) {}
    CFENCE();
}

DEV void waits(unsigned a, unsigned m) {
    CFENCE();
    while ((__builtin_amdgcn_readfirstlane(ldsrv<unsigned>(a)) & m) != m) __builtin_amdgcn_s_sleep(1);
    CFENCE();
}
DEV h2 oph(h2 x) { asm volatile("" : "+v"(x)); return x; }

DEV void put4(unsigned a, unsigned p01, unsigned p23) {
    unsigned p12 = __builtin_amdgcn_alignbit(p23, p01, 16);
    ldsw<u2>(a, (u2){p01, p23});
    ldsw<unsigned short>(a + CS + 2, (unsigned short)p01);
    ldsw<unsigned>(a + CS + 4, p12);
    ldsw<unsigned short>(a + CS + 8, (unsigned short)(p23 >> 16));
    ldsw<unsigned>(a + 2 * CS + 4, p01);
    ldsw<unsigned>(a + 2 * CS + 8, p23);
    ldsw<unsigned short>(a + 3 * CS + 6, (unsigned short)p01);
    ldsw<unsigned>(a + 3 * CS + 8, p12);
    ldsw<unsigned short>(a + 3 * CS + 12, (unsigned short)(p23 >> 16));
}

DEV void put4g(unsigned a, unsigned p01, unsigned p23) {
    unsigned p12 = __builtin_amdgcn_alignbit(p23, p01, 16);
    ldsw<u2>(a + 3 * CS + 16, (u2){p01, p23});
    ldsw<unsigned short>(a + 2, (unsigned short)p01);
    ldsw<unsigned>(a + 4, p12);
    ldsw<unsigned short>(a + 16, (unsigned short)(p23 >> 16));
    ldsw<unsigned>(a + CS + 4, p01);
    ldsw<unsigned>(a + CS + 16, p23);
    ldsw<unsigned short>(a + 2 * CS + 6, (unsigned short)p01);
    ldsw<unsigned>(a + 2 * CS + 16, p12);
    ldsw<unsigned short>(a + 2 * CS + 20, (unsigned short)(p23 >> 16));
}

struct Ctx {
    unsigned uf;
    unsigned ut;
    unsigned wt;
    unsigned fa, ga;
    unsigned sw;
    unsigned bd;
};

DEV u4 ufrag(unsigned uf, int S, int s0) { return ldsr<u4>(uf + 512 * S + 2 * (266 - s0)); }
DEV u4 gfrag(unsigned ga, int ytop, int d) { return ldsr<u4>(ga + 4 * (ytop + 1 - 16 * d)); }
DEV u4 ffrag(unsigned fa, int i) { return cat(ldsrv<u2>(fa + 544 - 64 * i), ldsrv<u2>(fa + 552 - 64 * i)); }
DEV u2 cv4(f4 d) { return (u2){pkbf(d[0], d[1]), pkbf(d[2], d[3])}; }
DEV u4 utr(const Ctx& c, int S, int p1, int p2) { return cat(ldtr(c.ut + 512 * S + 2 * (266 - p1)), ldtr(c.ut + 512 * S + 2 * (266 - p2))); }
DEV u4 wtr(const Ctx& c, int S, int p1, int p2) { return cat(ldtr(c.wt + 512 * S + 2 * (20 - p1)), ldtr(c.wt + 512 * S + 2 * (20 - p2))); }
#define KA(d) gfrag(c.ga, 47, d)
#define KB(d) gfrag(c.ga, 175, d)
#define KC(d) gfrag(c.ga, 119, d)
#define KD(d) gfrag(c.ga, 247, d)
#define KH(d) gfrag(c.ga, 383, d)
#define VA(S) utr(c, S, 18, 6)
#define VB(S) utr(c, S, 10, 14)
#define VC(S) wtr(c, S, 4, 12)
#define VD(S) wtr(c, S, 8, 16)
#define BAND(i) ldsr<u4>(c.bd + 1024 * (i))
DEV u4 bx2(u4 a, u4 b1, u4 a2, u4 b2) {
    const f4 z = {0.f, 0.f, 0.f, 0.f};
    return cat(cv4(mfb(a, b1, z)), cv4(mfb(a2, b2, z)));
}
DEV u4 and4(u4 m, unsigned v) { return (u4){m[0] & v, m[1] & v, m[2] & v, m[3] & v}; }

template <int R> DEV void near8(const Ctx& c, const u4* F, f4& a0, f4& a1) {
    u4 f0[8], f1[8];
#pragma unroll
    for (int i = 0; i < 6; i++) { f0[i] = ufrag(c.uf, R, 10 + 32 * i); f1[i] = ufrag(c.uf, R + 1, 10 + 32 * i); SB(); }
#pragma unroll
    for (int i = 0; i < 8; i++) {
        a0 = mfb(F[i], f0[i], a0); a1 = mfb(F[i], f1[i], a1);
        SB();
        if (i >= 1 && i < 3) { f0[i + 5] = ufrag(c.uf, R, 10 + 32 * (i + 5)); f1[i + 5] = ufrag(c.uf, R + 1, 10 + 32 * (i + 5)); SB(); }
    }
}
template <int R> DEV void near9(const Ctx& c, const u4* F, f4& a0, f4& a1) {
    u4 f0[9], f1[8];
#pragma unroll
    for (int i = 0; i < 4; i++) { f0[i] = ufrag(c.uf, R, 10 + 32 * i); f1[i] = ufrag(c.uf, R + 1, 10 + 32 * i); SB(); }
#pragma unroll
    for (int i = 0; i < 9; i++) {
        a0 = mfb(F[i], f0[i], a0);
        if (i < 8) a1 = mfb(F[i], f1[i], a1);
        if (i == 0) a1 = mfb(F[8], f0[0], a1);
        SB();
        if (i >= 1 && i < 6) { f0[i + 3] = ufrag(c.uf, R, 10 + 32 * (i + 3)); if (i < 5) f1[i + 3] = ufrag(c.uf, R + 1, 10 + 32 * (i + 3)); SB(); }
    }
}
template <int S> DEV void wbuild(const Ctx& c, u4 B1, unsigned ww) {
    const f4 z = {0.f, 0.f, 0.f, 0.f};
    const u4 n0 = ufrag(c.uf, S, 10);
    u4 n1;
    if (S < 6) n1 = ufrag(c.uf, S + 1, 10);
    ldsw<u2>(ww + 512 * S, cv4(mfb(B1, n0, z)));
    if (S < 6) ldsw<u2>(ww + 512 * (S + 1), cv4(mfb(B1, n1, z)));
}

#define PROF_OUT()
#define STG(pp) (((pp) >> 1) * 8 * T_STRIDE + ((pp) & 1) * 2048)
#define STAGE(pp, x0, x1) do { ldsw<u2>(st + ((pp) & 1) * 2048, cv4(x0)); ldsw<u2>(st + ((pp) & 1) * 2048 + 576, cv4(x1)); \
    arrive(cnt, 1u << (3 * (pp))); } while (0)
#define FINISH() do { PROF_OUT(); asm volatile("s_endpgm" ::: "memory"); } while (0)

__global__ __launch_bounds__(1024, 4) void s4d_kernel(const unsigned short* __restrict__ u, const unsigned short* __restrict__ lar,
                                                      const unsigned short* __restrict__ aim, const unsigned short* __restrict__ cre,
                                                      const unsigned short* __restrict__ cim, const unsigned short* __restrict__ ldt,
                                                      const unsigned short* __restrict__ dsk, unsigned short* __restrict__ out
) {
    __shared__ __attribute__((aligned(16))) char smem[LDS_BYTES];
    const unsigned sb = (unsigned)(uintptr_t)(AS3 char*)smem;
    STAMP(0);
    const int tid = threadIdx.x;
    l8 ka;
    unsigned cpf;
    {
        unsigned long long pc;
        asm volatile("s_load_dwordx16 %0, %2, 0x0\n"
                     "s_getpc_b64 %1" : "=&s"(ka), "=&s"(pc) : "s"(__builtin_amdgcn_kernarg_segment_ptr()));
        asm volatile("v_lshlrev_b32 %0, 7, %2\n"
                     "v_min_u32 %0, %3, %0\n"
                     "global_load_dword %0, %0, %1"
                     : "=&v"(cpf) : "s"(pc), "v"(tid), "i"(CODE_BYTES));
    }
    SB();
    const int lane = tid & 63;
    const int w = __builtin_amdgcn_readfirstlane(tid >> 6);
    const int c4 = w & 3, r = w >> 2;
    const int bx = blockIdx.x;
    const int xi = bx >> 3;
    const int gcnt = (int)(0x1818140c0c141818ull >> (8 * (bx & 7))) & 0xff, gb = (int)(0x88705c5044301800ull >> (8 * (bx & 7))) & 0xff;
    if (xi >= gcnt) { asm volatile("s_waitcnt vmcnt(0) lgkmcnt(0)\n s_endpgm" :: "v"(cpf), "s"(ka) : "memory"); }
    const int grp = gb + xi;
    const int ch = grp * 4 + c4;
    const int la = lane & 15, lg = lane >> 4;

    const unsigned ub = sb + U_OFF + c4 * U_STRIDE;
    const unsigned fgb = sb + FG_OFF + c4 * FG_STRIDE;
    const f4 zf = {0.f, 0.f, 0.f, 0.f};
    Ctx c;
#define COMMON_SETUP() \
    c.uf = opaque(ub + 32 * la + 16 * lg); \
    c.ut = opaque(ub + 8 * lane); \
    c.wt = opaque(sb + W_OFF + c4 * W_STRIDE + 8 * lane); \
    c.fa = opaque(fgb + F_REL + (CS + 2) * ((la + 3) & 3) - 2 * la + 16 * lg + 2); \
    c.ga = opaque(fgb + CS * (la & 3) - 4 * (la & 12) + 16 * lg); \
    c.bd = opaque(sb + B_OFF + 16 * lane);
    unsigned pl, pa, pr, pi, ldh, dkh, uo;
    const u4 z4 = {0u, 0u, 0u, 0u};
    const unsigned za = opaque(sb + FG_OFF + tid * 16);
    if (r == 3) {
        const int lt = tid & 255;
        ldsw<u4>(za + 12288, z4);
        ldsw<u4>(za + 16384 - 960, z4);
        const unsigned zj = min((unsigned)lt >> 2, 39u);
        const unsigned z1 = opaque(U_OFF + zj * 16), z2 = opaque(U_OFF + zj * 1024 + (528 - 33 * 1024)), z3 = opaque(W_OFF + (zj - 38) * 16);
        ldsw<u4>(sb + (lt & 3) * (zj < 38 ? U_STRIDE : W_STRIDE) + (zj < 34 ? z1 : (zj < 38 ? z2 : z3)), z4);
        SB();
        const unsigned idx = min(lt * gcnt + xi, 4095);
        const unsigned poff = opaque((idx >> 1) * 1280 + gb * 8 + (idx & 1) * (gcnt * 8 - 4));
        uo = opaque(lt * 2560 + grp * 8);
        KILL(pa); KILL(pr); KILL(pi); KILL(ldh); KILL(dkh);
        asm volatile("s_waitcnt lgkmcnt(0)" :: "s"(ka) : "memory");
        asm volatile("global_load_dword %0, %1, %2" : "=v"(pl) : "v"(poff), "s"(ka[0]));
    } else {
        ldsw<u4>(za, z4); ldsw<u4>(za + 12288, z4);
        const unsigned vc = opaque(2 * ch), vo = opaque(2 * (ch * NN + r * 64 + lane));
        if (w < 6) {
            const unsigned pat = (unsigned)((w < 4 ? 0x3F801F00001F00E0ull : 0xC07FC0ull) >> (16 * c4)) & 0xffffu;
            const unsigned val = w == 5 ? 0xffffu : 0x3F80u;
            const unsigned by = (pat << la >> (8 * lg)) & 0xffu;
            const unsigned e = by | (by << 15);
            u4 bq;
#pragma unroll
            for (int m = 0; m < 4; m++) bq[m] = __umul24((e >> (2 * m)) & 0x10001u, val);
            ldsw<u4>(za + (B_OFF - FG_OFF), bq);
        }
        ldh = opaque(0u); pl = opaque(0u); pa = opaque(0u); pr = opaque(0u); pi = opaque(0u); dkh = opaque(0u);
        SB();
        asm volatile("s_waitcnt lgkmcnt(0)" :: "s"(ka) : "memory");
        asm volatile("global_load_short_d16_hi %0, %1, %2" : "+v"(ldh) : "v"(vc), "s"(ka[5]));
        asm volatile("global_load_short_d16_hi %0, %1, %2" : "+v"(pl) : "v"(vo), "s"(ka[1]));
        asm volatile("global_load_short_d16_hi %0, %1, %2" : "+v"(pa) : "v"(vo), "s"(ka[2]));
        asm volatile("global_load_short_d16_hi %0, %1, %2" : "+v"(pr) : "v"(vo), "s"(ka[3]));
        asm volatile("global_load_short_d16_hi %0, %1, %2" : "+v"(pi) : "v"(vo), "s"(ka[4]));
        asm volatile("global_load_short_d16_hi %0, %1, %2" : "+v"(dkh) : "v"(vc), "s"(ka[6]));
        KILL(uo);
    }
    STAMP(1);
    asm volatile("s_barrier" ::: "memory");
    STAMP(2);
    SB();
    if (opaques(r) == 3) {
        __builtin_amdgcn_s_setprio(3);
        u2 ua[2], uc[2], ud[2];
#define ULOAD(x) \
        asm volatile("global_load_dwordx2 %0, %1, %2" : "=v"(x[0]) : "v"(uo), "s"(ka[0])); \
        asm volatile("global_load_dwordx2 %0, %1, %2 offset:1280" : "=v"(x[1]) : "v"(uo), "s"(ka[0])); \
        uo += 512 * 1280;
#define UW(cc, h, val) asm volatile("ds_write_addtid_b32 %0 offset:%1" :: "v"(val), "i"((cc) * U_STRIDE + 1024 * (h)) : "memory")
#define UPUT(x, h, n) \
        asm volatile("s_waitcnt vmcnt(" #n ")" : "+v"(x[0]), "+v"(x[1])); \
        UW(0, h, __builtin_amdgcn_perm(x[1][0], x[0][0], 0x05040100u)); \
        UW(1, h, __builtin_amdgcn_perm(x[1][0], x[0][0], 0x07060302u)); \
        UW(2, h, __builtin_amdgcn_perm(x[1][1], x[0][1], 0x05040100u)); \
        UW(3, h, __builtin_amdgcn_perm(x[1][1], x[0][1], 0x07060302u)); \
        arrive4(sw4, 1u << (12 + 3 * (h))); SB();
        ULOAD(ua);
        ULOAD(uc);
        SB();
        c.uf = opaque(ub + 32 * la + 16 * lg);
        c.sw = opaque(sb + MISC_OFF + 4 * c4);
        c.bd = opaque(sb + B_OFF + 16 * lane);
        const unsigned sw4 = opaque(sb + MISC_OFF + 4 * lane);
        const unsigned ww = opaque(sb + W_OFF + 32 + c4 * W_STRIDE + 32 * la + 8 * lg);
        const u4 B1 = BAND(0);
        asm volatile("s_mov_b32 m0, %0\n s_nop 0" :: "s"(sb + U_OFF + 532 + 256 * c4) : "memory");
        SB();
        ULOAD(ud);
        STAMP(3);
        UPUT(ua, 0, 4);
        asm volatile("" :: "v"(pl), "v"(cpf));
        STAMP(4);
        ULOAD(ua); SB();
        UPUT(uc, 1, 4);
        waitm(c.sw, S_U(0)); wbuild<0>(c, B1, ww); arrive(c.sw, S_W01); SB();
        UPUT(ud, 2, 2);
        STAMP(5);
        waitm(c.sw, S_U(1)); wbuild<2>(c, B1, ww); arrive(c.sw, S_W23); SB();
        UPUT(ua, 3, 0);
        STAMP(6);
        waitm(c.sw, S_U(2)); wbuild<4>(c, B1, ww); arrive(c.sw, S_W45); SB();
        waitm(c.sw, S_U(3)); wbuild<6>(c, B1, ww); arrive(c.sw, S_W6);
        STAMP(7);
        FINISH();
        KILL(cpf); KILL(pl); KILL(pa); KILL(pr); KILL(pi); KILL(ldh); KILL(dkh);
    }
    {
        __builtin_amdgcn_s_setprio(3);
        const unsigned tb = sb + w * T_STRIDE;
        const unsigned tr = opaque(tb + 288 * la + 16 * lg);
        const unsigned pw = opaque(sb + P_OFF + c4 * 6144 + 16 * lane);
        const unsigned pw0 = opaque(pw + (r >> 1) * 1024), pw2 = opaque(pw + (r & 1) * 1024);
        unsigned wk, wpt, st;
        c.sw = opaque(sb + MISC_OFF + 4 * c4);
        if (r == 1) {
            const unsigned u0 = __builtin_amdgcn_perm(0x6D492D48u, 0x6C092C08u, la & 7), u1 = __builtin_amdgcn_perm(0x24406401u, 0x25654100u, la & 7);
            wk = opaque(fgb + 8 * ((la < 8 ? u0 : u1) & 0xffu) + 16 * lg);
            const unsigned wa = opaque((CS + 2) * (lane & 3) + F_REL + 54 - 2 * (la >> 2)), wb = opaque(CS * (lane & 3) + ((lane & 3) == 3 ? 80 : 66 + 2 * (lane & 3)));
            wpt = opaque(fgb + (la < 12 ? wa : wb));
            KILL(st); KILL(c.uf); KILL(c.ut); KILL(c.wt); KILL(c.fa); KILL(c.ga); KILL(c.bd);
        } else {
            COMMON_SETUP();
            st = opaque(tb + 16 * c4 + 8 * la + 128 * lg + 64 * (lg >> 1));
            const unsigned u0 = __builtin_amdgcn_perm(0x80889098u, 0x5D1D5C1Cu, la & 7), u1 = __builtin_amdgcn_perm(0u, 0x81899199u, la & 3);
            const unsigned w2 = opaque(8 * ((la < 8 ? u0 : u1) & 0xffu) + 16 * lg), w0 = opaque(F_REL + 56 + 32 * la + 8 * lg);
            wk = opaque(fgb + (r == 0 ? w0 : w2));
            KILL(wpt);
        }
        const unsigned cnt = opaque(sb + MISC_OFF + 16);
        asm volatile("s_mov_b32 m0, %0\n s_nop 0" :: "s"(tb) : "memory");
        STAMP(3);
        asm volatile("s_waitcnt vmcnt(0)" : "+v"(ldh), "+v"(pl), "+v"(pa), "+v"(pr), "+v"(pi), "+v"(dkh) : "v"(cpf));
        const float LOG2E = 1.4426950408889634f;
        const float dt = __uint_as_float(pkbf(0.f, __builtin_amdgcn_exp2f(__uint_as_float(ldh) * LOG2E)));
        const float arp = __uint_as_float(pkbf(0.f, __builtin_amdgcn_exp2f(__uint_as_float(pl) * LOG2E)));
        const float m1 = arp * (-dt * LOG2E);
        const float th = __builtin_amdgcn_fractf(__uint_as_float(pa) * (dt * 0.15915494309189535f));
        const float mg = __builtin_amdgcn_exp2f(m1);
        f2 z = (f2){__builtin_amdgcn_cosf(th), __builtin_amdgcn_sinf(th)} * mg;
        STAMP(4);
        unsigned mx;
        asm("v_max_f32 %0, |%1|, |%2|\n s_nop 1" : "=v"(mx) : "v"(pr), "v"(pi));
#define MXS(ctl, rm) mx = max(mx, (unsigned)__builtin_amdgcn_update_dpp(0, (int)mx, ctl, rm, 0xf, true))
        const h2 e1 = pkh(z); MXS(0x111, 0xf); z = csq(z); SB();
        const h2 e2 = pkh(z); MXS(0x112, 0xf); z = csq(z); SB();
        const h2 e4 = pkh(z); MXS(0x114, 0xf); z = csq(z); SB();
        const h2 e8 = pkh(z);
        const h2 one = {(_Float16)1.f, (_Float16)0.f};
        h2 t0, t1, t2, t3;
        t0 = cmul(e2, e1); MXS(0x118, 0xf); t1 = cmul(e4, e1); TW(15, one); TW(14, e1); SB();
        const h2 y6 = cmul(e4, e2); MXS(0x142, 0xa); t2 = cmul(e8, e1); TW(13, e2); TW(11, e4); TW(7, e8); SB();
        const h2 y10 = cmul(e8, e2); MXS(0x143, 0xc); const h2 y12 = cmul(e8, e4); TW(12, t0); TW(10, t1); SB();
        const float th2 = th * 16.f;
        const float mg2 = __builtin_amdgcn_exp2f(m1 * 16.f);
        t0 = cmul(y6, e1); const h2 y14 = cmul(e8, y6); TW(9, y6); TW(6, t2); SB();
        z = (f2){__builtin_amdgcn_cosf(th2), __builtin_amdgcn_sinf(th2)} * mg2;
        const unsigned ex = max((unsigned)__builtin_amdgcn_readlane(mx, 63) >> 23, 16u);
        const float sc = __uint_as_float((268u - ex) << 23);
        const float ks = dt * __uint_as_float((ex - 13u) << 23);
        t1 = cmul(y10, e1); t2 = cmul(y12, e1); TW(5, y10); TW(3, y12); SB();
        t3 = cmul(y14, e1);
        TW(8, t0); TW(1, y14); SB();
        const h2 x0 = pkh((f2){__uint_as_float(pr), -__uint_as_float(pi)} * sc);
        const h2 e16 = pkh(z); z = csq(z);
        TW(4, t1); TW(2, t2); TW(0, t3);
        u4 yf[4];
#pragma unroll
        for (int s = 0; s < 4; s++) yf[s] = ldsr<u4>(tr + 64 * s);
        const h2 e32 = pkh(z); z = csq(z);
        SB();
        const h2 x1 = cmulc(x0, e16), x2 = cmulc(x0, e32);
        const h2 e64 = pkh(z); z = csq(z);
        TW(15, x0); SB();
        const h2 x3 = cmulc(x1, e32), x4 = cmulc(x0, e64);
        const h2 e128 = pkh(z); SB();
        const h2 x5 = cmulc(x1, e64), x6 = cmulc(x2, e64); TW(14, x1); TW(13, x2); SB();
        const h2 x7 = cmulc(x3, e64); t0 = cmulc(x0, e128); TW(12, x3); TW(11, x4); SB();
        t1 = cmulc(x1, e128); t2 = cmulc(x2, e128); TW(10, x5); TW(9, x6); SB();
        t3 = cmulc(x3, e128); h2 t4 = cmulc(x4, e128); TW(8, x7); TW(7, t0); SB();
        t0 = cmulc(x5, e128); h2 t5 = cmulc(x6, e128); TW(6, t1); TW(5, t2); SB();
        t1 = cmulc(x7, e128); TW(4, t3); TW(3, t4); SB();
        z = csq(z); TW(2, t0); TW(1, t5); SB();
        TW(0, t1);
        u4 xf[4];
#pragma unroll
        for (int s = 0; s < 4; s++) xf[s] = ldsr<u4>(tr + 64 * s);
        const h2 e256 = pkh(z); z = csq(z);
        const h2 p272 = cmul(e256, e16); t0 = cmul(e256, e4);
        SB();
        t1 = cmul(e256, y6); const h2 p264 = cmul(e256, e8); SB();
        f4 acc0 = zf, acc1 = zf, acc2 = zf;
#pragma unroll
        for (int s = 0; s < 4; s++) acc0 = mfh(yf[s], xf[s], acc0);
        t2 = cmul(e256, y10); t3 = cmul(e256, y12); TW(7, p272); TW(1, t0); SB();
        t4 = cmul(e256, y14); t5 = cmul(p272, e2); TW(2, t1); TW(3, p264); SB();
        const h2 e512 = pkh(z); z = csq(z);
        acc0 *= ks;
        STAMP(5);
        asm volatile("s_cmp_eq_u32 %0, 0\n s_cbranch_scc1 .Lp0_%=\n ds_write_b128 %1, %2\n s_mov_b64 exec, 1\n ds_add_u32 %3, %4\n s_mov_b64 exec, -1\n"
                     ".Lp0_%=:\n s_cmp_eq_u32 %0, 2\n s_cbranch_scc1 .Lp1_%=\n s_setprio 2\n .Lp1_%=:"
                     :: "s"(r), "v"(pw0), "v"(acc0), "v"(c.sw), "v"(1u) : "scc", "memory");
        SB();
        const h2 p520 = cmul(e512, e8), p528 = cmul(e512, e16); TW(4, t2); TW(5, t3); SB();
        const h2 p516 = cmul(e512, e4), p524 = cmul(e512, y12); TW(6, t4); TW(0, t5); SB();
        const h2 e1024 = pkh(z);
        t4 = cmul(e256, e2); TW(12, p520); TW(8, p528); TW(9, e256); SB();
        const h2 p776 = cmul(e256, p520), p784 = cmul(e256, p528); TW(15, p516); TW(11, p524); TW(10, t4);
        SB();
#pragma unroll
        for (int s = 0; s < 4; s++) yf[s] = ldsr<u4>(tr + 64 * s);
        t0 = cmul(e256, p516); t1 = cmul(e256, p524);
        t2 = cmul(e1024, e8); t3 = cmul(e1024, p264); TW(1, p776); TW(3, p784); SB();
#pragma unroll
        for (int s = 0; s < 4; s++) acc1 = mfh(xf[s], yf[s], acc1);
        t4 = cmul(e1024, p520); t5 = cmul(e1024, p776); TW(0, t0); TW(2, t1); SB();
        t0 = cmul(e1024, e16); t1 = cmul(e1024, p272); TW(4, t2); TW(5, t3); SB();
        t2 = cmul(e1024, p528); t3 = cmul(e1024, p784); TW(6, t4); TW(7, t5); SB();
        TW(8, t0); TW(9, t1); TW(10, t2); TW(11, t3);
        SB();
#pragma unroll
        for (int s = 0; s < 4; s++) yf[s] = ldsr<u4>(tr + 64 * s);
        acc1 *= ks;
        SB();
        if (r != 1) { ldsw<f4>(pw0 + 2048, acc1); arrive(c.sw, 4u); }
        KSTAMP(8);
        SB();
#pragma unroll
        for (int s = 0; s < 4; s++) acc2 = mfh(xf[s], yf[s], acc2);
        acc2 *= ks;
        if (r == 2) {
            waitm(c.sw, S_C2);
            f4 kk = acc2 + ldsr<f4>(pw + 4096) + ldsr<f4>(pw + 5120);
            const unsigned q01 = pkbf(kk[0], kk[1]), q23 = pkbf(kk[2], kk[3]);
            if (la < 12) put4g(wk, q01, q23);
            const unsigned k1024 = (__builtin_amdgcn_readlane(q01, 3) & 0xffffu) * 0x10001u;
            arrive(c.sw, S_G2);
            __builtin_amdgcn_s_setprio(1);
            STAMP(6);
            f4 a[4] = {zf, zf, zf, zf};
            u4 p[5], v[5], k[4];
            {
                const u4 B3 = BAND(3), B4 = BAND(4), BF = and4(BAND(5), k1024);
#pragma unroll
                for (int d = 0; d < 4; d++) k[d] = KH(4 + d);
                waits(c.sw, S_U(0) | S_U(1));
                u4 q[4], n[4];
#pragma unroll
                for (int S = 0; S < 4; S++) { p[S] = ufrag(c.uf, S, 18); q[S] = ufrag(c.uf, S, 26); }
#pragma unroll
                for (int S = 0; S < 4; S++) n[S] = ufrag(c.uf, S, 10);
                SB();
#pragma unroll
                for (int S = 0; S < 4; S++) v[S] = bx2(p[S], B3, q[S], B4);
#pragma unroll
                for (int t = 0; t < 4; t++) a[t] = mfb(BF, n[t], a[t]);
            }
            const u4 B2a = BAND(1), B2b = BAND(2);
            SB();
#pragma unroll
            for (int S = 0; S < 4; S++)
#pragma unroll
                for (int t = S; t < 4; t++) a[t] = mfb(v[S], k[t - S], a[t]);
#pragma unroll
            for (int S = 0; S < 4; S++) v[S] = bx2(p[S], B2b, p[S], B2a);
            waits(c.sw, S_G1 | S_W45);
            STAMP(7);
            p[4] = ufrag(c.uf, 4, 18);
            const u4 kd3 = KD(3), kd4 = KD(4);
            u4 vc[6];
#pragma unroll
            for (int S = 0; S < 6; S++) vc[S] = VC(S);
#pragma unroll
            for (int d = 0; d < 4; d++) k[d] = KC(4 - d);
            SB();
            v[4] = bx2(p[4], B2b, p[4], B2a);
#pragma unroll
            for (int t = 0; t < 4; t++) a[t] = mfb(v[t], kd4, a[t]);
#pragma unroll
            for (int t = 0; t < 4; t++) a[t] = mfb(v[t + 1], kd3, a[t]);
            SB();
            u4 k1[4], k2;
#pragma unroll
            for (int S = 0; S < 4; S++) v[S] = VA(2 + S);
            k2 = KA(2); k1[0] = KA(1);
            SB();
#pragma unroll
            for (int d = 0; d < 4; d++)
#pragma unroll
                for (int t = 0; t < 4; t++) if (t + d < 6) a[t] = mfb(vc[t + d], k[d], a[t]);
            k1[2] = k[3];
            SB();
#pragma unroll
            for (int S = 0; S < 4; S++) p[S] = VB(2 + S);
            k[0] = KB(2); k1[1] = KB(1);
            SB();
#pragma unroll
            for (int t = 0; t < 4; t++) a[t] = mfb(v[t], k2, a[t]);
#pragma unroll
            for (int t = 0; t < 3; t++) a[t] = mfb(v[t + 1], k1[0], a[t]);
            SB();
#pragma unroll
            for (int S = 0; S < 4; S++) v[S] = VD(2 + S);
            k2 = KD(2); k1[3] = KD(1);
            SB();
#pragma unroll
            for (int t = 0; t < 4; t++) a[t] = mfb(p[t], k[0], a[t]);
#pragma unroll
            for (int t = 0; t < 3; t++) a[t] = mfb(p[t + 1], k1[1], a[t]);
            SB();
            waitm(c.sw, S_F);
            u4 F[9];
#pragma unroll
            for (int i = 0; i < 9; i++) F[i] = ffrag(c.fa, i);
            SB();
#pragma unroll
            for (int t = 0; t < 4; t++) a[t] = mfb(v[t], k2, a[t]);
#pragma unroll
            for (int t = 0; t < 3; t++) a[t] = mfb(v[t + 1], k1[3], a[t]);
            STAMP(8);
            near9<4>(c, F, a[0], a[1]);
            STAGE(2, a[0], a[1]);
            STAMP(9);
            waitm(c.sw, S_W6);
            {
                const u4 va = VA(6), vb = VB(6), vc6 = VC(6), vd = VD(6);
                SB();
                near9<6>(c, F, a[2], a[3]);
                a[3] = mfb(va, k1[0], a[3]); a[3] = mfb(vb, k1[1], a[3]); a[3] = mfb(vc6, k1[2], a[3]); a[3] = mfb(vd, k1[3], a[3]);
            }
            STAGE(3, a[2], a[3]);
            STAMP(10);
            FINISH();
            KILL(acc0); KILL(acc1); KILL(acc2); KILL(wk); KILL(wpt); KILL(dkh);
        }
        ldsw<f4>(pw2 + 4096, acc2);
        arrive(c.sw, 16u);
        __builtin_amdgcn_s_setprio(3);
        KSTAMP(7);
        if (r == 1) {
            waitm(c.sw, S_C1);
            f4 kk = acc1 + ldsr<f4>(pw + 2048) + ldsr<f4>(pw + 3072);
            unsigned q01 = pkbf(kk[0], kk[1]), q23 = pkbf(kk[2], kk[3]);
            const unsigned k256 = __builtin_amdgcn_readlane(q23, 57) >> 16;
            const unsigned k258 = __builtin_amdgcn_readlane(q23, 58) >> 16;
            const unsigned k512 = __builtin_amdgcn_readlane(q01, 7);
            q01 = __builtin_amdgcn_update_dpp(q01, q01, 0xC0, 0xf, 0xc, false);
            q23 = __builtin_amdgcn_update_dpp(q23, q23, 0xC0, 0xf, 0xc, false);
            put4g(wk, q01, q23);
            CFENCE();
            ldsw<unsigned short>(wpt, (unsigned short)(la < 8 ? k256 : (la < 12 ? k258 : k512)));
            arrive(c.sw, S_G1);
            STAMP(6);
            const unsigned ea = opaque(sb + ((lane >> 2) & 3) * (T_STRIDE + 16) + 8 * lg + 128 * (lane & 3) + 32 * (lane & 2) + 64 * (c4 & 1) + 576 * (c4 >> 1));
            auto ro = __builtin_amdgcn_make_buffer_rsrc((void*)ka[7], (short)0, 2048 * 1280, 0x00020000);
            const unsigned vo = opaque((lane + 128 * c4) * 1280 + grp * 8);
#pragma unroll
            for (int pp = 0; pp < 4; pp++) {
                if (pp < 3) waits(cnt, 4u << (3 * pp)); else waitm(cnt, 4u << (3 * pp));
                const u2 v0 = ldtr(ea + STG(pp)), v1 = ldtr(ea + STG(pp) + 32);
                __builtin_amdgcn_raw_buffer_store_b64(v0, ro, vo, 512 * pp * 1280, 0);
                __builtin_amdgcn_raw_buffer_store_b64(v1, ro, vo, (512 * pp + 64) * 1280, 0);
                STAMP(7 + pp);
            }
            FINISH();
            KILL(acc0); KILL(wk); KILL(dkh);
        }
        {
            waitm(c.sw, S_C0);
            f4 kk = acc0 + ldsr<f4>(pw) + ldsr<f4>(pw + 1024);
            kk[3] += lane == 63 ? __uint_as_float(dkh) : 0.f;
            put4(wk, pkbf(kk[0], kk[1]), pkbf(kk[2], kk[3]));
            arrive(c.sw, S_F);
            __builtin_amdgcn_s_setprio(XPRIO);
            STAMP(6);
            f4 a[4] = {zf, zf, zf, zf};
            u4 F[8];
#pragma unroll
            for (int i = 0; i < 8; i++) F[i] = ffrag(c.fa, i);
            waits(c.sw, S_U(0));
            STAMP(7);
            near8<0>(c, F, a[0], a[1]);
            waits(c.sw, S_U(1));
            near8<2>(c, F, a[2], a[3]);
            u4 n[4];
#pragma unroll
            for (int t = 0; t < 4; t++) n[t] = ufrag(c.uf, t, 266);
            const u4 va0 = VA(0), vb0 = VB(0);
            waits(c.sw, S_G1 | S_W01);
            STAMP(8);
            __builtin_amdgcn_s_setprio(2);
            const u4 F8 = ffrag(c.fa, 8), ka1 = KA(1), kb1 = KB(1), vc0 = VC(0), kc1 = KC(1), vd0 = VD(0), kd1 = KD(1);
            SB();
            const u4 va1 = VA(1), vb1 = VB(1), vc1 = VC(1), vd1 = VD(1), ka2 = KA(2), kb2 = KB(2), kc2 = KC(2), kd2 = KD(2);
            SB();
            a[1] = mfb(va0, ka1, a[1]); a[0] = mfb(F8, n[0], a[0]); a[1] = mfb(vb0, kb1, a[1]); a[1] = mfb(F8, n[1], a[1]);
            a[1] = mfb(vc0, kc1, a[1]); a[1] = mfb(vd0, kd1, a[1]);
            a[2] = mfb(F8, n[2], a[2]); a[3] = mfb(F8, n[3], a[3]);
            STAGE(0, a[0], a[1]);
            STAMP(9);
            const u4 B2a = BAND(1), B2b = BAND(2), p0 = ufrag(c.uf, 0, 18), va2 = VA(2), vb2 = VB(2);
            SB();
            a[2] = mfb(va1, ka1, a[2]); a[3] = mfb(va1, ka2, a[3]); a[2] = mfb(va0, ka2, a[2]);
            a[2] = mfb(vb1, kb1, a[2]); a[3] = mfb(vb1, kb2, a[3]); a[2] = mfb(vb0, kb2, a[2]);
            waitm(c.sw, S_G2 | S_W23);
            const u4 vc2 = VC(2), vd2 = VD(2), kc3 = KC(3), kd3 = KD(3);
            SB();
            const u4 w0 = bx2(p0, B2b, p0, B2a);
            a[2] = mfb(vc1, kc1, a[2]); a[3] = mfb(vc1, kc2, a[3]); a[2] = mfb(vc0, kc2, a[2]);
            a[2] = mfb(vd1, kd1, a[2]); a[3] = mfb(vd1, kd2, a[3]); a[2] = mfb(vd0, kd2, a[2]);
            a[3] = mfb(va2, ka1, a[3]); a[3] = mfb(vb2, kb1, a[3]); a[3] = mfb(vc2, kc1, a[3]); a[3] = mfb(vd2, kd1, a[3]);
            a[3] = mfb(vc0, kc3, a[3]); a[3] = mfb(w0, kd3, a[3]);
            STAGE(1, a[2], a[3]);
            STAMP(10);
            FINISH();
        }
    }
}

extern "C" void kernel_launch(void* const* d_in, const int* in_sizes, int n_in, void* d_out, int out_size, void* d_ws, size_t ws_size,
                              hipStream_t stream) {
    s4d_kernel<<<192, 1024, 0, stream>>>((const unsigned short*)d_in[0], (const unsigned short*)d_in[1], (const unsigned short*)d_in[2],
                                         (const unsigned short*)d_in[3], (const unsigned short*)d_in[4], (const unsigned short*)d_in[5],
                                         (const unsigned short*)d_in[6], (unsigned short*)d_out
    );
}
```

```cpp
#include <hip/hip_runtime.h>
#include <stdint.h>

typedef _Float16 h2 __attribute__((ext_vector_type(2)));
typedef _Float16 h8 __attribute__((ext_vector_type(8)));
typedef __bf16 b2 __attribute__((ext_vector_type(2)));
typedef __bf16 b8 __attribute__((ext_vector_type(8)));
typedef float f2 __attribute__((ext_vector_type(2)));
typedef float f4 __attribute__((ext_vector_type(4)));
typedef unsigned u2 __attribute__((ext_vector_type(2)));
typedef unsigned u4 __attribute__((ext_vector_type(4)));
typedef unsigned long long l8 __attribute__((ext_vector_type(8)));
typedef short s4 __attribute__((ext_vector_type(4)));
#define AS3 __attribute__((address_space(3)))
#define DEV __device__ __forceinline__
#define SB() __builtin_amdgcn_sched_barrier(0)
#define KILL(x) asm volatile("" : "=v"(x))
#define CFENCE() asm volatile("" ::: "memory")

#define NN 192
#define XPRIO 1
#define CODE_BYTES 12288

#define T_STRIDE 4608
#define U_OFF 55296
#define U_STRIDE 4640
#define FG_OFF 73856
#define CS 1984
#define FG_STRIDE (4 * CS)
#define F_REL 1360
#define MISC_OFF (FG_OFF + 4 * FG_STRIDE)
#define W_OFF (MISC_OFF + 64)
#define W_STRIDE 3648
#define P_OFF (W_OFF + 4 * W_STRIDE)
#define B_OFF (P_OFF + 4 * 6144)
#define LDS_BYTES (B_OFF + 6144)
#define S_C0 2u
#define S_C1 8u
#define S_C2 32u
#define S_F 64u
#define S_G1 128u
#define S_G2 256u
#define S_W01 512u
#define S_W23 1024u
#define S_W45 2048u
#define S_U(h) (4u << (12 + 3 * (h)))
#define S_W6 (1u << 24)

template <class T> DEV T ldsr(unsigned a) { return *(const AS3 T*)(uintptr_t)(a); }
template <class T> DEV T ldsrv(unsigned a) { return *(const volatile AS3 T*)(uintptr_t)(a); }
template <class T> DEV void ldsw(unsigned a, T v) { *(AS3 T*)(uintptr_t)(a) = v; }
DEV u2 ldtr(unsigned a) { s4 v = __builtin_amdgcn_ds_read_tr16_b64_v4i16((AS3 s4*)(uintptr_t)(a)); return __builtin_bit_cast(u2, v); }
DEV int opaques(int x) { asm volatile("" : "+s"(x)); return x; }
DEV unsigned opaque(unsigned x) { asm volatile("" : "+v"(x)); return x; }

DEV unsigned pkbf(float a, float b) { f2 v = {a, b}; return __builtin_bit_cast(unsigned, __builtin_convertvector(v, b2)); }
DEV h2 cmul(h2 h, h2 e) { h2 t = h * (h2){e.x, e.x}; return __builtin_elementwise_fma((h2){-h.y, h.x}, (h2){e.y, e.y}, t); }
DEV h2 cmulc(h2 h, h2 e) { h2 t = h * (h2){e.x, e.x}; return __builtin_elementwise_fma((h2){h.y, -h.x}, (h2){e.y, e.y}, t); }
DEV f4 mfb(u4 a, u4 b, f4 c) { return __builtin_amdgcn_mfma_f32_16x16x32_bf16(__builtin_bit_cast(b8, a), __builtin_bit_cast(b8, b), c, 0, 0, 0); }
DEV f4 mfh(u4 a, u4 b, f4 c) { return __builtin_amdgcn_mfma_f32_16x16x32_f16(__builtin_bit_cast(h8, a), __builtin_bit_cast(h8, b), c, 0, 0, 0); }
DEV u4 cat(u2 a, u2 b) { return (u4){a.x, a.y, b.x, b.y}; }

#define TW(row, val) asm volatile("ds_write_addtid_b32 %0 offset:%1" :: "v"(val), "i"((row) * 288) : "memory")

DEV f2 csq(f2 z) {
    f2 t, r;
    asm("v_pk_mul_f32 %0, %1, %1 op_sel_hi:[0,1]" : "=v"(t) : "v"(z));
    asm("v_pk_fma_f32 %0, %1, %1, %2 op_sel:[1,1,0] op_sel_hi:[0,1,1] neg_lo:[1,0,0]" : "=v"(r) : "v"(z), "v"(t));
    return r;
}
DEV h2 pkh(f2 v) { return __builtin_convertvector(v, h2); }

DEV void arrive(unsigned a, unsigned v) {
    asm volatile("s_mov_b64 exec, 1\n ds_add_u32 %0, %1\n s_mov_b64 exec, -1" :: "v"(a), "v"(v) : "memory");
}
DEV void arrive4(unsigned a, unsigned v) {
    asm volatile("s_mov_b64 exec, 15\n ds_add_u32 %0, %1\n s_mov_b64 exec, -1" :: "v"(a), "v"(v) : "memory");
}
DEV void waitm(unsigned a, unsigned m) {
    CFENCE();
    while ((__builtin_amdgcn_readfirstlane(ldsrv<unsigned>(a)) & m) != m) {}
    CFENCE();
}

DEV void waits(unsigned a, unsigned m) {
    CFENCE();
    while ((__builtin_amdgcn_readfirstlane(ldsrv<unsigned>(a)) & m) != m) __builtin_amdgcn_s_sleep(1);
    CFENCE();
}
DEV h2 oph(h2 x) { asm volatile("" : "+v"(x)); return x; }

DEV void put4(unsigned a, unsigned p01, unsigned p23) {
    unsigned p12 = __builtin_amdgcn_alignbit(p23, p01, 16);
    ldsw<u2>(a, (u2){p01, p23});
    ldsw<unsigned short>(a + CS + 2, (unsigned short)p01);
    ldsw<unsigned>(a + CS + 4, p12);
    ldsw<unsigned short>(a + CS + 8, (unsigned short)(p23 >> 16));
    ldsw<unsigned>(a + 2 * CS + 4, p01);
    ldsw<unsigned>(a + 2 * CS + 8, p23);
    ldsw<unsigned short>(a + 3 * CS + 6, (unsigned short)p01);
    ldsw<unsigned>(a + 3 * CS + 8, p12);
    ldsw<unsigned short>(a + 3 * CS + 12, (unsigned short)(p23 >> 16));
}

DEV void put4g(unsigned a, unsigned p01, unsigned p23) {
    unsigned p12 = __builtin_amdgcn_alignbit(p23, p01, 16);
    ldsw<u2>(a + 3 * CS + 16, (u2){p01, p23});
    ldsw<unsigned short>(a + 2, (unsigned short)p01);
    ldsw<unsigned>(a + 4, p12);
    ldsw<unsigned short>(a + 16, (unsigned short)(p23 >> 16));
    ldsw<unsigned>(a + CS + 4, p01);
    ldsw<unsigned>(a + CS + 16, p23);
    ldsw<unsigned short>(a + 2 * CS + 6, (unsigned short)p01);
    ldsw<unsigned>(a + 2 * CS + 16, p12);
    ldsw<unsigned short>(a + 2 * CS + 20, (unsigned short)(p23 >> 16));
}

struct Ctx {
    unsigned uf;
    unsigned ut;
    unsigned wt;
    unsigned fa, ga;
    unsigned sw;
    unsigned bd;
};

DEV u4 ufrag(unsigned uf, int S, int s0) { return ldsr<u4>(uf + 512 * S + 2 * (266 - s0)); }
DEV u4 gfrag(unsigned ga, int ytop, int d) { return ldsr<u4>(ga + 4 * (ytop + 1 - 16 * d)); }
DEV u4 ffrag(unsigned fa, int i) { return cat(ldsrv<u2>(fa + 544 - 64 * i), ldsrv<u2>(fa + 552 - 64 * i)); }
DEV u2 cv4(f4 d) { return (u2){pkbf(d[0], d[1]), pkbf(d[2], d[3])}; }
DEV u4 utr(const Ctx& c, int S, int p1, int p2) { return cat(ldtr(c.ut + 512 * S + 2 * (266 - p1)), ldtr(c.ut + 512 * S + 2 * (266 - p2))); }
DEV u4 wtr(const Ctx& c, int S, int p1, int p2) { return cat(ldtr(c.wt + 512 * S + 2 * (20 - p1)), ldtr(c.wt + 512 * S + 2 * (20 - p2))); }
#define KA(d) gfrag(c.ga, 47, d)
#define KB(d) gfrag(c.ga, 175, d)
#define KC(d) gfrag(c.ga, 119, d)
#define KD(d) gfrag(c.ga, 247, d)
#define KH(d) gfrag(c.ga, 383, d)
#define VA(S) utr(c, S, 18, 6)
#define VB(S) utr(c, S, 10, 14)
#define VC(S) wtr(c, S, 4, 12)
#define VD(S) wtr(c, S, 8, 16)
#define BAND(i) ldsr<u4>(c.bd + 1024 * (i))
DEV u4 bx2(u4 a, u4 b1, u4 a2, u4 b2) {
    const f4 z = {0.f, 0.f, 0.f, 0.f};
    return cat(cv4(mfb(a, b1, z)), cv4(mfb(a2, b2, z)));
}
DEV u4 and4(u4 m, unsigned v) { return (u4){m[0] & v, m[1] & v, m[2] & v, m[3] & v}; }

template <int R> DEV void near8(const Ctx& c, const u4* F, f4& a0, f4& a1) {
    u4 f0[8], f1[8];
#pragma unroll
    for (int i = 0; i < 3; i++) { f0[i] = ufrag(c.uf, R, 10 + 32 * i); f1[i] = ufrag(c.uf, R + 1, 10 + 32 * i); }
    SB();
#pragma unroll
    for (int i = 3; i < 6; i++) { f0[i] = ufrag(c.uf, R, 10 + 32 * i); f1[i] = ufrag(c.uf, R + 1, 10 + 32 * i); }
#pragma unroll
    for (int i = 0; i < 3; i++) { a0 = mfb(F[i], f0[i], a0); a1 = mfb(F[i], f1[i], a1); }
    SB();
#pragma unroll
    for (int i = 6; i < 8; i++) { f0[i] = ufrag(c.uf, R, 10 + 32 * i); f1[i] = ufrag(c.uf, R + 1, 10 + 32 * i); }
#pragma unroll
    for (int i = 3; i < 6; i++) { a0 = mfb(F[i], f0[i], a0); a1 = mfb(F[i], f1[i], a1); }
    SB();
#pragma unroll
    for (int i = 6; i < 8; i++) { a0 = mfb(F[i], f0[i], a0); a1 = mfb(F[i], f1[i], a1); }
}
template <int R> DEV void near9(const Ctx& c, const u4* F, f4& a0, f4& a1) {
    u4 f0[9], f1[8];
#pragma unroll
    for (int i = 0; i < 3; i++) { f0[i] = ufrag(c.uf, R, 10 + 32 * i); f1[i] = ufrag(c.uf, R + 1, 10 + 32 * i); }
    SB();
#pragma unroll
    for (int i = 3; i < 6; i++) { f0[i] = ufrag(c.uf, R, 10 + 32 * i); f1[i] = ufrag(c.uf, R + 1, 10 + 32 * i); }
#pragma unroll
    for (int i = 0; i < 3; i++) { a0 = mfb(F[i], f0[i], a0); a1 = mfb(F[i], f1[i], a1); }
    a1 = mfb(F[8], f0[0], a1);
    SB();
#pragma unroll
    for (int i = 6; i < 9; i++) { f0[i] = ufrag(c.uf, R, 10 + 32 * i); if (i < 8) f1[i] = ufrag(c.uf, R + 1, 10 + 32 * i); }
#pragma unroll
    for (int i = 3; i < 6; i++) { a0 = mfb(F[i], f0[i], a0); a1 = mfb(F[i], f1[i], a1); }
    SB();
#pragma unroll
    for (int i = 6; i < 9; i++) { a0 = mfb(F[i], f0[i], a0); if (i < 8) a1 = mfb(F[i], f1[i], a1); }
}
template <int S> DEV void wbuild(const Ctx& c, u4 B1, unsigned ww) {
    const f4 z = {0.f, 0.f, 0.f, 0.f};
    const u4 n0 = ufrag(c.uf, S, 10);
    u4 n1;
    if (S < 6) n1 = ufrag(c.uf, S + 1, 10);
    ldsw<u2>(ww + 512 * S, cv4(mfb(B1, n0, z)));
    if (S < 6) ldsw<u2>(ww + 512 * (S + 1), cv4(mfb(B1, n1, z)));
}

#define STG(pp) (((pp) >> 1) * 8 * T_STRIDE + ((pp) & 1) * 2048)
#define STAGE(pp, x0, x1) do { ldsw<u2>(st + ((pp) & 1) * 2048, cv4(x0)); ldsw<u2>(st + ((pp) & 1) * 2048 + 576, cv4(x1)); \
    arrive(cnt, 1u << (3 * (pp))); } while (0)
#define FINISH() do { asm volatile("s_endpgm" ::: "memory"); } while (0)

__global__ __launch_bounds__(1024, 4) void s4d_kernel(const unsigned short* __restrict__ u, const unsigned short* __restrict__ lar,
                                                      const unsigned short* __restrict__ aim, const unsigned short* __restrict__ cre,
                                                      const unsigned short* __restrict__ cim, const unsigned short* __restrict__ ldt,
                                                      const unsigned short* __restrict__ dsk, unsigned short* __restrict__ out
) {
    __shared__ __attribute__((aligned(16))) char smem[LDS_BYTES];
    const unsigned sb = (unsigned)(uintptr_t)(AS3 char*)smem;
    const int tid = threadIdx.x;
    l8 ka;
    unsigned cpf;
    {
        unsigned long long pc;
        asm volatile("s_load_dwordx16 %0, %2, 0x0\n"
                     "s_getpc_b64 %1" : "=&s"(ka), "=&s"(pc) : "s"(__builtin_amdgcn_kernarg_segment_ptr()));
        asm volatile("v_lshlrev_b32 %0, 7, %2\n"
                     "v_min_u32 %0, %3, %0\n"
                     "global_load_dword %0, %0, %1"
                     : "=&v"(cpf) : "s"(pc), "v"(tid), "i"(CODE_BYTES));
    }
    SB();
    const int lane = tid & 63;
    const int w = __builtin_amdgcn_readfirstlane(tid >> 6);
    const int c4 = w & 3, r = w >> 2;
    const int bx = blockIdx.x;
    const int xi = bx >> 3;
    const int gcnt = (int)(0x1818140c0c141818ull >> (8 * (bx & 7))) & 0xff, gb = (int)(0x88705c5044301800ull >> (8 * (bx & 7))) & 0xff;
    if (xi >= gcnt) { asm volatile("s_waitcnt vmcnt(0) lgkmcnt(0)\n s_endpgm" :: "v"(cpf), "s"(ka) : "memory"); }
    const int grp = gb + xi;
    const int ch = grp * 4 + c4;
    const int la = lane & 15, lg = lane >> 4;

    const unsigned ub = sb + U_OFF + c4 * U_STRIDE;
    const unsigned fgb = sb + FG_OFF + c4 * FG_STRIDE;
    const f4 zf = {0.f, 0.f, 0.f, 0.f};
    Ctx c;
#define COMMON_SETUP() \
    c.uf = opaque(ub + 32 * la + 16 * lg); \
    c.ut = opaque(ub + 8 * lane); \
    c.wt = opaque(sb + W_OFF + c4 * W_STRIDE + 8 * lane); \
    c.fa = opaque(fgb + F_REL + (CS + 2) * ((la + 3) & 3) - 2 * la + 16 * lg + 2); \
    c.ga = opaque(fgb + CS * (la & 3) - 4 * (la & 12) + 16 * lg); \
    c.bd = opaque(sb + B_OFF + 16 * lane);
    unsigned pl, pa, pr, pi, ldh, dkh, uo;
    const u4 z4 = {0u, 0u, 0u, 0u};
    const unsigned za = opaque(sb + FG_OFF + tid * 16);
    if (r == 3) {
        const int lt = tid & 255;
        ldsw<u4>(za + 12288, z4);
        ldsw<u4>(za + 16384 - 960, z4);
        const unsigned zj = min((unsigned)lt >> 2, 39u);
        const unsigned z1 = opaque(U_OFF + zj * 16), z2 = opaque(U_OFF + zj * 1024 + (528 - 33 * 1024)), z3 = opaque(W_OFF + (zj - 38) * 16);
        ldsw<u4>(sb + (lt & 3) * (zj < 38 ? U_STRIDE : W_STRIDE) + (zj < 34 ? z1 : (zj < 38 ? z2 : z3)), z4);
        SB();
        const unsigned idx = min(lt * gcnt + xi, 4095);
        const unsigned poff = opaque((idx >> 1) * 1280 + gb * 8 + (idx & 1) * (gcnt * 8 - 4));
        uo = opaque(lt * 2560 + grp * 8);
        KILL(pa); KILL(pr); KILL(pi); KILL(ldh); KILL(dkh);
        asm volatile("s_waitcnt lgkmcnt(0)" :: "s"(ka) : "memory");
        asm volatile("global_load_dword %0, %1, %2" : "=v"(pl) : "v"(poff), "s"(ka[0]));
    } else {
        ldsw<u4>(za, z4); ldsw<u4>(za + 12288, z4);
        const unsigned vc = opaque(2 * ch), vo = opaque(2 * (ch * NN + r * 64 + lane));
        if (w < 6) {
            const unsigned pat = (unsigned)((w < 4 ? 0x3F801F00001F00E0ull : 0xC07FC0ull) >> (16 * c4)) & 0xffffu;
            const unsigned val = w == 5 ? 0xffffu : 0x3F80u;
            const unsigned by = (pat << la >> (8 * lg)) & 0xffu;
            const unsigned e = by | (by << 15);
            u4 bq;
#pragma unroll
            for (int m = 0; m < 4; m++) bq[m] = __umul24((e >> (2 * m)) & 0x10001u, val);
            ldsw<u4>(za + (B_OFF - FG_OFF), bq);
        }
        ldh = opaque(0u); pl = opaque(0u); pa = opaque(0u); pr = opaque(0u); pi = opaque(0u); dkh = opaque(0u);
        SB();
        asm volatile("s_waitcnt lgkmcnt(0)" :: "s"(ka) : "memory");
        asm volatile("global_load_short_d16_hi %0, %1, %2" : "+v"(ldh) : "v"(vc), "s"(ka[5]));
        asm volatile("global_load_short_d16_hi %0, %1, %2" : "+v"(pl) : "v"(vo), "s"(ka[1]));
        asm volatile("global_load_short_d16_hi %0, %1, %2" : "+v"(pa) : "v"(vo), "s"(ka[2]));
        asm volatile("global_load_short_d16_hi %0, %1, %2" : "+v"(pr) : "v"(vo), "s"(ka[3]));
        asm volatile("global_load_short_d16_hi %0, %1, %2" : "+v"(pi) : "v"(vo), "s"(ka[4]));
        asm volatile("global_load_short_d16_hi %0, %1, %2" : "+v"(dkh) : "v"(vc), "s"(ka[6]));
        KILL(uo);
    }
    asm volatile("s_barrier" ::: "memory");
    SB();
    if (opaques(r) == 3) {
        __builtin_amdgcn_s_setprio(3);
        u2 ua[2], uc[2], ud[2];
#define ULOAD(x) \
        asm volatile("global_load_dwordx2 %0, %1, %2" : "=v"(x[0]) : "v"(uo), "s"(ka[0])); \
        asm volatile("global_load_dwordx2 %0, %1, %2 offset:1280" : "=v"(x[1]) : "v"(uo), "s"(ka[0])); \
        uo += 512 * 1280;
#define UW(cc, h, val) asm volatile("ds_write_addtid_b32 %0 offset:%1" :: "v"(val), "i"((cc) * U_STRIDE + 1024 * (h)) : "memory")
#define UPUT(x, h, n) \
        asm volatile("s_waitcnt vmcnt(" #n ")" : "+v"(x[0]), "+v"(x[1])); \
        UW(0, h, __builtin_amdgcn_perm(x[1][0], x[0][0], 0x05040100u)); \
        UW(1, h, __builtin_amdgcn_perm(x[1][0], x[0][0], 0x07060302u)); \
        UW(2, h, __builtin_amdgcn_perm(x[1][1], x[0][1], 0x05040100u)); \
        UW(3, h, __builtin_amdgcn_perm(x[1][1], x[0][1], 0x07060302u)); \
        arrive4(sw4, 1u << (12 + 3 * (h))); SB();
        ULOAD(ua);
        ULOAD(uc);
        SB();
        c.uf = opaque(ub + 32 * la + 16 * lg);
        c.sw = opaque(sb + MISC_OFF + 4 * c4);
        c.bd = opaque(sb + B_OFF + 16 * lane);
        const unsigned sw4 = opaque(sb + MISC_OFF + 4 * lane);
        const unsigned ww = opaque(sb + W_OFF + 32 + c4 * W_STRIDE + 32 * la + 8 * lg);
        const u4 B1 = BAND(0);
        asm volatile("s_mov_b32 m0, %0\n s_nop 0" :: "s"(sb + U_OFF + 532 + 256 * c4) : "memory");
        SB();
        ULOAD(ud);
        UPUT(ua, 0, 4);
        asm volatile("" :: "v"(pl), "v"(cpf));
        ULOAD(ua); SB();
        UPUT(uc, 1, 4);
        waitm(c.sw, S_U(0)); wbuild<0>(c, B1, ww); arrive(c.sw, S_W01); SB();
        UPUT(ud, 2, 2);
        waitm(c.sw, S_U(1)); wbuild<2>(c, B1, ww); arrive(c.sw, S_W23); SB();
        UPUT(ua, 3, 0);
        waitm(c.sw, S_U(2)); wbuild<4>(c, B1, ww); arrive(c.sw, S_W45); SB();
        waitm(c.sw, S_U(3)); wbuild<6>(c, B1, ww); arrive(c.sw, S_W6);
        FINISH();
        KILL(cpf); KILL(pl); KILL(pa); KILL(pr); KILL(pi); KILL(ldh); KILL(dkh);
    }
    {
        __builtin_amdgcn_s_setprio(3);
        const unsigned tb = sb + w * T_STRIDE;
        const unsigned tr = opaque(tb + 288 * la + 16 * lg);
        const unsigned pw = opaque(sb + P_OFF + c4 * 6144 + 16 * lane);
        const unsigned pw0 = opaque(pw + (r >> 1) * 1024), pw2 = opaque(pw + (r & 1) * 1024);
        unsigned wk, wpt, st;
        c.sw = opaque(sb + MISC_OFF + 4 * c4);
        if (r == 1) {
            const unsigned u0 = __builtin_amdgcn_perm(0x6D492D48u, 0x6C092C08u, la & 7), u1 = __builtin_amdgcn_perm(0x24406401u, 0x25654100u, la & 7);
            wk = opaque(fgb + 8 * ((la < 8 ? u0 : u1) & 0xffu) + 16 * lg);
            const unsigned wa = opaque((CS + 2) * (lane & 3) + F_REL + 54 - 2 * (la >> 2)), wb = opaque(CS * (lane & 3) + ((lane & 3) == 3 ? 80 : 66 + 2 * (lane & 3)));
            wpt = opaque(fgb + (la < 12 ? wa : wb));
            KILL(st); KILL(c.uf); KILL(c.ut); KILL(c.wt); KILL(c.fa); KILL(c.ga); KILL(c.bd);
        } else {
            COMMON_SETUP();
            st = opaque(tb + 16 * c4 + 8 * la + 128 * lg + 64 * (lg >> 1));
            const unsigned u0 = __builtin_amdgcn_perm(0x80889098u, 0x5D1D5C1Cu, la & 7), u1 = __builtin_amdgcn_perm(0u, 0x81899199u, la & 3);
            const unsigned w2 = opaque(8 * ((la < 8 ? u0 : u1) & 0xffu) + 16 * lg), w0 = opaque(F_REL + 56 + 32 * la + 8 * lg);
            wk = opaque(fgb + (r == 0 ? w0 : w2));
            KILL(wpt);
        }
        const unsigned cnt = opaque(sb + MISC_OFF + 16);
        asm volatile("s_mov_b32 m0, %0\n s_nop 0" :: "s"(tb) : "memory");
        asm volatile("s_waitcnt vmcnt(0)" : "+v"(ldh), "+v"(pl), "+v"(pa), "+v"(pr), "+v"(pi), "+v"(dkh) : "v"(cpf));
        const float LOG2E = 1.4426950408889634f;
        const float dt = __uint_as_float(pkbf(0.f, __builtin_amdgcn_exp2f(__uint_as_float(ldh) * LOG2E)));
        const float arp = __uint_as_float(pkbf(0.f, __builtin_amdgcn_exp2f(__uint_as_float(pl) * LOG2E)));
        const float m1 = arp * (-dt * LOG2E);
        const float th = __builtin_amdgcn_fractf(__uint_as_float(pa) * (dt * 0.15915494309189535f));
        const float mg = __builtin_amdgcn_exp2f(m1);
        f2 z = (f2){__builtin_amdgcn_cosf(th), __builtin_amdgcn_sinf(th)} * mg;
        unsigned mx;
        asm("v_max_f32 %0, |%1|, |%2|\n s_nop 1" : "=v"(mx) : "v"(pr), "v"(pi));
#define MXS(ctl, rm) mx = max(mx, (unsigned)__builtin_amdgcn_update_dpp(0, (int)mx, ctl, rm, 0xf, true))
        const h2 e1 = pkh(z); MXS(0x111, 0xf); z = csq(z); SB();
        const h2 e2 = pkh(z); MXS(0x112, 0xf); z = csq(z); SB();
        const h2 e4 = pkh(z); MXS(0x114, 0xf); z = csq(z); SB();
        const h2 e8 = pkh(z);
        const h2 one = {(_Float16)1.f, (_Float16)0.f};
        h2 t0, t1, t2, t3;
        t0 = cmul(e2, e1); MXS(0x118, 0xf); t1 = cmul(e4, e1); TW(15, one); TW(14, e1); SB();
        const h2 y6 = cmul(e4, e2); MXS(0x142, 0xa); t2 = cmul(e8, e1); TW(13, e2); TW(11, e4); TW(7, e8); SB();
        const h2 y10 = cmul(e8, e2); MXS(0x143, 0xc); const h2 y12 = cmul(e8, e4); TW(12, t0); TW(10, t1); SB();
        const float th2 = th * 16.f;
        const float mg2 = __builtin_amdgcn_exp2f(m1 * 16.f);
        t0 = cmul(y6, e1); const h2 y14 = cmul(e8, y6); TW(9, y6); TW(6, t2); SB();
        z = (f2){__builtin_amdgcn_cosf(th2), __builtin_amdgcn_sinf(th2)} * mg2;
        const unsigned ex = max((unsigned)__builtin_amdgcn_readlane(mx, 63) >> 23, 16u);
        const float sc = __uint_as_float((268u - ex) << 23);
        const float ks = dt * __uint_as_float((ex - 13u) << 23);
        t1 = cmul(y10, e1); t2 = cmul(y12, e1); TW(5, y10); TW(3, y12); SB();
        t3 = cmul(y14, e1);
        TW(8, t0); TW(1, y14); SB();
        const h2 x0 = pkh((f2){__uint_as_float(pr), -__uint_as_float(pi)} * sc);
        const h2 e16 = pkh(z); z = csq(z);
        TW(4, t1); TW(2, t2); TW(0, t3);
        u4 yf[4];
#pragma unroll
        for (int s = 0; s < 4; s++) yf[s] = ldsr<u4>(tr + 64 * s);
        const h2 e32 = pkh(z); z = csq(z);
        SB();
        const h2 x1 = cmulc(x0, e16), x2 = cmulc(x0, e32);
        const h2 e64 = pkh(z); z = csq(z);
        TW(15, x0); SB();
        const h2 x3 = cmulc(x1, e32), x4 = cmulc(x0, e64);
        const h2 e128 = pkh(z); SB();
        const h2 x5 = cmulc(x1, e64), x6 = cmulc(x2, e64); TW(14, x1); TW(13, x2); SB();
        const h2 x7 = cmulc(x3, e64); t0 = cmulc(x0, e128); TW(12, x3); TW(11, x4); SB();
        t1 = cmulc(x1, e128); t2 = cmulc(x2, e128); TW(10, x5); TW(9, x6); SB();
        t3 = cmulc(x3, e128); h2 t4 = cmulc(x4, e128); TW(8, x7); TW(7, t0); SB();
        t0 = cmulc(x5, e128); h2 t5 = cmulc(x6, e128); TW(6, t1); TW(5, t2); SB();
        t1 = cmulc(x7, e128); TW(4, t3); TW(3, t4); SB();
        z = csq(z); TW(2, t0); TW(1, t5); SB();
        TW(0, t1);
        u4 xf[4];
#pragma unroll
        for (int s = 0; s < 4; s++) xf[s] = ldsr<u4>(tr + 64 * s);
        const h2 e256 = pkh(z); z = csq(z);
        const h2 p272 = cmul(e256, e16); t0 = cmul(e256, e4);
        SB();
        t1 = cmul(e256, y6); const h2 p264 = cmul(e256, e8); SB();
        f4 acc0 = zf, acc1 = zf, acc2 = zf;
#pragma unroll
        for (int s = 0; s < 4; s++) acc0 = mfh(yf[s], xf[s], acc0);
        t2 = cmul(e256, y10); t3 = cmul(e256, y12); TW(7, p272); TW(1, t0); SB();
        t4 = cmul(e256, y14); t5 = cmul(p272, e2); TW(2, t1); TW(3, p264); SB();
        const h2 e512 = pkh(z); z = csq(z);
        acc0 *= ks;
        asm volatile("s_cmp_eq_u32 %0, 0\n s_cbranch_scc1 .Lp0_%=\n ds_write_b128 %1, %2\n s_mov_b64 exec, 1\n ds_add_u32 %3, %4\n s_mov_b64 exec, -1\n"
                     ".Lp0_%=:\n s_cmp_eq_u32 %0, 2\n s_cbranch_scc1 .Lp1_%=\n s_setprio 2\n .Lp1_%=:"
                     :: "s"(r), "v"(pw0), "v"(acc0), "v"(c.sw), "v"(1u) : "scc", "memory");
        SB();
        const h2 p520 = cmul(e512, e8), p528 = cmul(e512, e16); TW(4, t2); TW(5, t3); SB();
        const h2 p516 = cmul(e512, e4), p524 = cmul(e512, y12); TW(6, t4); TW(0, t5); SB();
        const h2 e1024 = pkh(z);
        t4 = cmul(e256, e2); TW(12, p520); TW(8, p528); TW(9, e256); SB();
        const h2 p776 = cmul(e256, p520), p784 = cmul(e256, p528); TW(15, p516); TW(11, p524); TW(10, t4);
        SB();
#pragma unroll
        for (int s = 0; s < 4; s++) yf[s] = ldsr<u4>(tr + 64 * s);
        t0 = cmul(e256, p516); t1 = cmul(e256, p524);
        t2 = cmul(e1024, e8); t3 = cmul(e1024, p264); TW(1, p776); TW(3, p784); SB();
#pragma unroll
        for (int s = 0; s < 4; s++) acc1 = mfh(xf[s], yf[s], acc1);
        t4 = cmul(e1024, p520); t5 = cmul(e1024, p776); TW(0, t0); TW(2, t1); SB();
        t0 = cmul(e1024, e16); t1 = cmul(e1024, p272); TW(4, t2); TW(5, t3); SB();
        t2 = cmul(e1024, p528); t3 = cmul(e1024, p784); TW(6, t4); TW(7, t5); SB();
        TW(8, t0); TW(9, t1); TW(10, t2); TW(11, t3);
        SB();
#pragma unroll
        for (int s = 0; s < 4; s++) yf[s] = ldsr<u4>(tr + 64 * s);
        acc1 *= ks;
        SB();
        if (r != 1) { ldsw<f4>(pw0 + 2048, acc1); arrive(c.sw, 4u); }
        SB();
#pragma unroll
        for (int s = 0; s < 4; s++) acc2 = mfh(xf[s], yf[s], acc2);
        acc2 *= ks;
        if (r == 2) {
            waitm(c.sw, S_C2);
            f4 kk = acc2 + ldsr<f4>(pw + 4096) + ldsr<f4>(pw + 5120);
            const unsigned q01 = pkbf(kk[0], kk[1]), q23 = pkbf(kk[2], kk[3]);
            if (la < 12) put4g(wk, q01, q23);
            const unsigned k1024 = (__builtin_amdgcn_readlane(q01, 3) & 0xffffu) * 0x10001u;
            arrive(c.sw, S_G2);
            __builtin_amdgcn_s_setprio(1);
            f4 a[4] = {zf, zf, zf, zf};
            u4 p[5], v[5], k[4];
            {
                const u4 B3 = BAND(3), B4 = BAND(4), BF = and4(BAND(5), k1024);
#pragma unroll
                for (int d = 0; d < 4; d++) k[d] = KH(4 + d);
                waits(c.sw, S_U(0) | S_U(1));
                u4 q[4], n[4];
#pragma unroll
                for (int S = 0; S < 4; S++) { p[S] = ufrag(c.uf, S, 18); q[S] = ufrag(c.uf, S, 26); }
#pragma unroll
                for (int S = 0; S < 4; S++) n[S] = ufrag(c.uf, S, 10);
                SB();
#pragma unroll
                for (int S = 0; S < 4; S++) v[S] = bx2(p[S], B3, q[S], B4);
#pragma unroll
                for (int t = 0; t < 4; t++) a[t] = mfb(BF, n[t], a[t]);
            }
            const u4 B2a = BAND(1), B2b = BAND(2);
            SB();
#pragma unroll
            for (int S = 0; S < 4; S++)
#pragma unroll
                for (int t = S; t < 4; t++) a[t] = mfb(v[S], k[t - S], a[t]);
#pragma unroll
            for (int S = 0; S < 4; S++) v[S] = bx2(p[S], B2b, p[S], B2a);
            waits(c.sw, S_G1 | S_W45);
            p[4] = ufrag(c.uf, 4, 18);
            const u4 kd3 = KD(3), kd4 = KD(4);
            u4 vc[6];
#pragma unroll
            for (int S = 0; S < 6; S++) vc[S] = VC(S);
#pragma unroll
            for (int d = 0; d < 4; d++) k[d] = KC(4 - d);
            SB();
            v[4] = bx2(p[4], B2b, p[4], B2a);
#pragma unroll
            for (int t = 0; t < 4; t++) a[t] = mfb(v[t], kd4, a[t]);
#pragma unroll
            for (int t = 0; t < 4; t++) a[t] = mfb(v[t + 1], kd3, a[t]);
            SB();
            u4 k1[4], k2;
#pragma unroll
            for (int S = 0; S < 4; S++) v[S] = VA(2 + S);
            k2 = KA(2); k1[0] = KA(1);
#pragma unroll
            for (int d = 0; d < 4; d++)
#pragma unroll
                for (int t = 0; t < 4; t++) if (t + d < 6) a[t] = mfb(vc[t + d], k[d], a[t]);
            k1[2] = k[3];
            SB();
#pragma unroll
            for (int S = 0; S < 4; S++) p[S] = VB(2 + S);
            k[0] = KB(2); k1[1] = KB(1);
#pragma unroll
            for (int t = 0; t < 4; t++) a[t] = mfb(v[t], k2, a[t]);
#pragma unroll
            for (int t = 0; t < 3; t++) a[t] = mfb(v[t + 1], k1[0], a[t]);
            SB();
#pragma unroll
            for (int S = 0; S < 4; S++) v[S] = VD(2 + S);
            k2 = KD(2); k1[3] = KD(1);
#pragma unroll
            for (int t = 0; t < 4; t++) a[t] = mfb(p[t], k[0], a[t]);
#pragma unroll
            for (int t = 0; t < 3; t++) a[t] = mfb(p[t + 1], k1[1], a[t]);
            SB();
            waitm(c.sw, S_F);
            u4 F[9];
#pragma unroll
            for (int i = 0; i < 9; i++) F[i] = ffrag(c.fa, i);
#pragma unroll
            for (int t = 0; t < 4; t++) a[t] = mfb(v[t], k2, a[t]);
#pragma unroll
            for (int t = 0; t < 3; t++) a[t] = mfb(v[t + 1], k1[3], a[t]);
            near9<4>(c, F, a[0], a[1]);
            STAGE(2, a[0], a[1]);
            waitm(c.sw, S_W6);
            near9<6>(c, F, a[2], a[3]);
            {
                const u4 va = VA(6), vb = VB(6), vc6 = VC(6), vd = VD(6);
                a[3] = mfb(va, k1[0], a[3]); a[3] = mfb(vb, k1[1], a[3]); a[3] = mfb(vc6, k1[2], a[3]); a[3] = mfb(vd, k1[3], a[3]);
            }
            STAGE(3, a[2], a[3]);
            FINISH();
            KILL(acc0); KILL(acc1); KILL(acc2); KILL(wk); KILL(wpt); KILL(dkh);
        }
        ldsw<f4>(pw2 + 4096, acc2);
        arrive(c.sw, 16u);
        __builtin_amdgcn_s_setprio(3);
        if (r == 1) {
            waitm(c.sw, S_C1);
            f4 kk = acc1 + ldsr<f4>(pw + 2048) + ldsr<f4>(pw + 3072);
            unsigned q01 = pkbf(kk[0], kk[1]), q23 = pkbf(kk[2], kk[3]);
            const unsigned k256 = __builtin_amdgcn_readlane(q23, 57) >> 16;
            const unsigned k258 = __builtin_amdgcn_readlane(q23, 58) >> 16;
            const unsigned k512 = __builtin_amdgcn_readlane(q01, 7);
            q01 = __builtin_amdgcn_update_dpp(q01, q01, 0xC0, 0xf, 0xc, false);
            q23 = __builtin_amdgcn_update_dpp(q23, q23, 0xC0, 0xf, 0xc, false);
            put4g(wk, q01, q23);
            CFENCE();
            ldsw<unsigned short>(wpt, (unsigned short)(la < 8 ? k256 : (la < 12 ? k258 : k512)));
            arrive(c.sw, S_G1);
            const unsigned ea = opaque(sb + ((lane >> 2) & 3) * (T_STRIDE + 16) + 8 * lg + 128 * (lane & 3) + 32 * (lane & 2) + 64 * (c4 & 1) + 576 * (c4 >> 1));
            auto ro = __builtin_amdgcn_make_buffer_rsrc((void*)ka[7], (short)0, 2048 * 1280, 0x00020000);
            const unsigned vo = opaque((lane + 128 * c4) * 1280 + grp * 8);
#pragma unroll
            for (int pp = 0; pp < 4; pp++) {
                waits(cnt, 4u << (3 * pp));
                const u2 v0 = ldtr(ea + STG(pp)), v1 = ldtr(ea + STG(pp) + 32);
                __builtin_amdgcn_raw_buffer_store_b64(v0, ro, vo, 512 * pp * 1280, 0);
                __builtin_amdgcn_raw_buffer_store_b64(v1, ro, vo, (512 * pp + 64) * 1280, 0);
            }
            FINISH();
            KILL(acc0); KILL(wk); KILL(dkh);
        }
        {
            waitm(c.sw, S_C0);
            f4 kk = acc0 + ldsr<f4>(pw) + ldsr<f4>(pw + 1024);
            kk[3] += lane == 63 ? __uint_as_float(dkh) : 0.f;
            put4(wk, pkbf(kk[0], kk[1]), pkbf(kk[2], kk[3]));
            arrive(c.sw, S_F);
            __builtin_amdgcn_s_setprio(XPRIO);
            f4 a[4] = {zf, zf, zf, zf};
            u4 F[8];
#pragma unroll
            for (int i = 0; i < 8; i++) F[i] = ffrag(c.fa, i);
            waits(c.sw, S_U(0));
            near8<0>(c, F, a[0], a[1]);
            waits(c.sw, S_U(1));
            near8<2>(c, F, a[2], a[3]);
            u4 n[4];
#pragma unroll
            for (int t = 0; t < 4; t++) n[t] = ufrag(c.uf, t, 266);
            const u4 va0 = VA(0), vb0 = VB(0);
            waits(c.sw, S_G1 | S_W01);
            __builtin_amdgcn_s_setprio(2);
            const u4 F8 = ffrag(c.fa, 8), ka1 = KA(1), kb1 = KB(1), vc0 = VC(0), kc1 = KC(1), vd0 = VD(0), kd1 = KD(1);
            SB();
            const u4 va1 = VA(1), vb1 = VB(1), vc1 = VC(1), vd1 = VD(1), ka2 = KA(2), kb2 = KB(2), kc2 = KC(2), kd2 = KD(2);
            a[1] = mfb(va0, ka1, a[1]); a[0] = mfb(F8, n[0], a[0]); a[1] = mfb(vb0, kb1, a[1]); a[1] = mfb(F8, n[1], a[1]);
            a[1] = mfb(vc0, kc1, a[1]); a[1] = mfb(vd0, kd1, a[1]);
            a[2] = mfb(F8, n[2], a[2]); a[3] = mfb(F8, n[3], a[3]);
            STAGE(0, a[0], a[1]);
            const u4 B2a = BAND(1), B2b = BAND(2), p0 = ufrag(c.uf, 0, 18), va2 = VA(2), vb2 = VB(2);
            a[2] = mfb(va1, ka1, a[2]); a[3] = mfb(va1, ka2, a[3]); a[2] = mfb(va0, ka2, a[2]);
            a[2] = mfb(vb1, kb1, a[2]); a[3] = mfb(vb1, kb2, a[3]); a[2] = mfb(vb0, kb2, a[2]);
            waitm(c.sw, S_G2 | S_W23);
            const u4 vc2 = VC(2), vd2 = VD(2), kc3 = KC(3), kd3 = KD(3);
            const u4 w0 = bx2(p0, B2b, p0, B2a);
            a[2] = mfb(vc1, kc1, a[2]); a[3] = mfb(vc1, kc2, a[3]); a[2] = mfb(vc0, kc2, a[2]);
            a[2] = mfb(vd1, kd1, a[2]); a[3] = mfb(vd1, kd2, a[3]); a[2] = mfb(vd0, kd2, a[2]);
            a[3] = mfb(va2, ka1, a[3]); a[3] = mfb(vb2, kb1, a[3]); a[3] = mfb(vc2, kc1, a[3]); a[3] = mfb(vd2, kd1, a[3]);
            a[3] = mfb(vc0, kc3, a[3]); a[3] = mfb(w0, kd3, a[3]);
            STAGE(1, a[2], a[3]);
            FINISH();
        }
    }
}

extern "C" void kernel_launch(void* const* d_in, const int* in_sizes, int n_in, void* d_out, int out_size, void* d_ws, size_t ws_size,
                              hipStream_t stream) {
    s4d_kernel<<<192, 1024, 0, stream>>>((const unsigned short*)d_in[0], (const unsigned short*)d_in[1], (const unsigned short*)d_in[2],
                                         (const unsigned short*)d_in[3], (const unsigned short*)d_in[4], (const unsigned short*)d_in[5],
                                         (const unsigned short*)d_in[6], (unsigned short*)d_out
    );
}
```
